```python
import torch

def _softmax(x, axis=-1):
    return torch.softmax(x, dim=axis)

def _gelu(x):
    return torch.nn.functional.gelu(x, approximate='tanh')

def _silu(x):
    return torch.nn.functional.silu(x)

def _sigmoid(x):
    return torch.sigmoid(x)

def _layernorm(x, axis=-1, eps=1e-05):
    m = x.mean(dim=axis, keepdim=True)
    v = x.var(dim=axis, keepdim=True, unbiased=False)
    return (x - m) / torch.sqrt(v + eps)

def _scatter_add(tgt, idx, src, axis=0):
    return tgt.clone().index_add_(axis, idx, src)

def _segment_sum(src, seg, n):
    out = torch.zeros((n,) + src.shape[1:], dtype=src.dtype)
    return out.index_add_(0, seg, src)

def _pad(x, pw):
    flat = tuple((v for pair in reversed(pw) for v in pair))
    return torch.nn.functional.pad(x, flat)
kernel_name = 'adaLN_cond__ssm_conv_h3__swiglu_ffn__prompt'

def setup_inputs():

    _w = lambda a: (lambda t: t.to(torch.bfloat16) if t.is_floating_point() else t)(torch.as_tensor(a))

    def _A_setup():
        B, S, D = (1, 2048, 448)
        ks = 4
        return {'q': _w((lambda *s: torch.randn(*s, dtype=torch.bfloat16) * 0.05)(B, S, D)), 'k': _w((lambda *s: torch.randn(*s, dtype=torch.bfloat16) * 1.6)(B, S, D)), 'v': _w((lambda *s: torch.randn(*s, dtype=torch.bfloat16) * 1.6)(B, S, D)), 'shift_k': _w((lambda *s: torch.randn(*s, dtype=torch.bfloat16) * 1.6)(D, ks) * 0.5), 'ssm_k': _w((lambda *s: torch.randn(*s, dtype=torch.bfloat16) * 1.6)(D, S) * (1.0 / S ** 0.5)), 'ssm_D': _w((lambda *s: torch.randn(*s, dtype=torch.bfloat16) * 1.6)(D))}
    _A_in = _A_setup()

    def _B_setup():
        B, S, D, F = (1, 2048, 448, 1792)
        return {'x': _w((lambda *s: torch.randn(*s, dtype=torch.bfloat16) * 0.05)(B, S, D)), 'w_gate': _w((lambda *s: torch.randn(*s, dtype=torch.bfloat16) * 0.1)(D, F)), 'w_up': _w((lambda *s: torch.randn(*s, dtype=torch.bfloat16) * 0.1)(D, F)), 'w_down': _w((lambda *s: torch.randn(*s, dtype=torch.bfloat16) * 0.1)(F, D))}
    _B_in = _B_setup()
    return {'A_q': _A_in['q'], 'A_k': _A_in['k'], 'A_v': _A_in['v'], 'A_shift_k': _A_in['shift_k'], 'A_ssm_k': _A_in['ssm_k'], 'A_ssm_D': _A_in['ssm_D'], 'B_x': _B_in['x'], 'B_w_gate': _B_in['w_gate'], 'B_w_up': _B_in['w_up'], 'B_w_down': _B_in['w_down'], 'c': _w((lambda *s: torch.randn(*s, dtype=torch.bfloat16) * 0.2)(1, 448) * 8.0), 'w_mod': _w((lambda *s: torch.randn(*s, dtype=torch.bfloat16) * 0.2)(448, 6 * 448) * 448 ** (-0.5) * 8.0)}

def reference(A_q, A_k, A_v, A_shift_k, A_ssm_k, A_ssm_D, B_x, B_w_gate, B_w_up, B_w_down, c, w_mod):

    def _A_ref(q, k, v, shift_k, ssm_k, ssm_D):
        B, S, D = q.shape
        ks = shift_k.shape[-1]
        k_pad = _pad(k, ((0, 0), (ks - 1, 0), (0, 0)))
        k_shift = torch.zeros_like(k)
        for t in range(ks):
            k_shift = k_shift + k_pad[:, t:t + S, :] * shift_k[:, ks - 1 - t][None, None, :]
        kv = k_shift * v
        L = 2 * S
        kv_pad = _pad(kv, ((0, 0), (0, S), (0, 0)))
        kf_pad = _pad(ssm_k, ((0, 0), (0, S)))
        kv_f = (lambda x, axes=(-2, -1): torch.fft.rfft2(x.float(), dim=axes))(kv_pad[:, None, :, :], axes=(1, 2))
        kf_f = (lambda x, axes=(-2, -1): torch.fft.rfft2(x.float(), dim=axes))(kf_pad[:, None, :], axes=(1, 2))
        kf_f = (lambda x, *d: x.permute(*d))(kf_f, 1, 2, 0)
        y_f = kv_f * kf_f[None, :, :, :]
        y = (lambda x, s, axes=(-2, -1): torch.fft.irfft2(x, s=s, dim=axes).to(torch.bfloat16))(y_f, (1, L), axes=(1, 2))[:, 0, :S, :]
        y = y + kv * ssm_D[None, None, :]
        return q * y

    def _B_ref(x, w_gate, w_up, w_down):
        g = _silu(x @ w_gate)
        u = x @ w_up
        return g * u @ w_down
    x = A_q
    mod = (_silu(c) @ w_mod).reshape(c.shape[0], 6, 1, -1)
    sa, ca, ga, sb, cb, gb = (mod[:, 0].clone(), mod[:, 1].clone(), mod[:, 2].clone(), mod[:, 3].clone(), mod[:, 4].clone(), mod[:, 5].clone())
    h = _layernorm(x, axis=-1) * (1 + ca) + sa
    x = x + ga * _A_ref(h, A_k, A_v, A_shift_k, A_ssm_k, A_ssm_D)
    h = _layernorm(x, axis=-1) * (1 + cb) + sb
    x = x + gb * _B_ref(h, B_w_gate, B_w_up, B_w_down)
    return x
```

```cpp
#include <hip/hip_runtime.h>
#include <hip/hip_bf16.h>
#include <stdint.h>

typedef unsigned short u16;
static constexpr int S = 2048, D = 448, F = 1792;

__device__ __forceinline__ float bf2f(u16 v) { return __uint_as_float(((unsigned)v) << 16); }
__device__ __forceinline__ u16 f2bf(float f) {
    __hip_bfloat16 b = __float2bfloat16(f);
    return *reinterpret_cast<u16*>(&b);
}
__device__ __forceinline__ float rbf(float f) { return bf2f(f2bf(f)); }

__global__ void k_mod(const u16* c, const u16* wmod, float* mod) {
    int j = blockIdx.x * blockDim.x + threadIdx.x;
    if (j >= 6 * D) return;
    float acc = 0.f;
    for (int i = 0; i < D; ++i) {
        float cv = bf2f(c[i]);
        float s = rbf(cv / (1.f + __expf(-cv)));
        acc += s * bf2f(wmod[(size_t)i * 6 * D + j]);
    }
    mod[j] = rbf(acc);
}

__global__ void k_kv(const u16* k, const u16* v, const u16* shk, u16* kvT) {
    int idx = blockIdx.x * blockDim.x + threadIdx.x;
    if (idx >= S * D) return;
    int s = idx / D, d = idx % D;
    float acc = 0.f;
    for (int t = 0; t < 4; ++t) {
        int r = s + t - 3;
        float kv = r >= 0 ? bf2f(k[(size_t)r * D + d]) : 0.f;
        acc = rbf(acc + rbf(kv * bf2f(shk[d * 4 + 3 - t])));
    }
    kvT[(size_t)d * S + s] = f2bf(acc * bf2f(v[idx]));
}

__global__ void k_conv(const u16* kvT, const u16* ssmk, const u16* ssmD, u16* y) {
    __shared__ float su[S], sk[S];
    int d = blockIdx.x;
    for (int i = threadIdx.x; i < S; i += blockDim.x) { su[i] = bf2f(kvT[(size_t)d * S + i]); sk[i] = bf2f(ssmk[(size_t)d * S + i]); }
    __syncthreads();
    float Dv = bf2f(ssmD[d]);
    for (int s = threadIdx.x; s < S; s += blockDim.x) {
        float acc = 0.f;
        for (int j = 0; j <= s; ++j) acc += su[s - j] * sk[j];
        float yy = rbf(acc);
        yy = rbf(yy + rbf(su[s] * Dv));
        y[(size_t)s * D + d] = f2bf(yy);
    }
}

__device__ float block_sum(float v, float* red) {
    for (int o = 32; o > 0; o >>= 1) v += __shfl_xor(v, o);
    __syncthreads();
    if ((threadIdx.x & 63) == 0) red[threadIdx.x >> 6] = v;
    __syncthreads();
    float r = 0.f;
    for (int i = 0; i < (int)(blockDim.x >> 6); ++i) r += red[i];
    return r;
}

__global__ void k_row(const u16* q, const u16* y, const float* mod, u16* x1o, u16* h2o) {
    __shared__ float red[16];
    int s = blockIdx.x, d = threadIdx.x;
    float x = bf2f(q[(size_t)s * D + d]);
    float m = block_sum(x, red) / D;
    float var = block_sum((x - m) * (x - m), red) / D;
    float mb = rbf(m), vb = rbf(var);
    float den = rbf(sqrtf(rbf(vb + 1e-5f)));
    float ln = rbf(rbf(x - mb) / den);
    float sa = mod[d], ca = mod[D + d], ga = mod[2 * D + d], sb = mod[3 * D + d], cb = mod[4 * D + d];
    float h = rbf(rbf(ln * rbf(1.f + ca)) + sa);
    float a = rbf(h * bf2f(y[(size_t)s * D + d]));
    float x1 = rbf(x + rbf(ga * a));
    m = block_sum(x1, red) / D;
    var = block_sum((x1 - m) * (x1 - m), red) / D;
    mb = rbf(m); vb = rbf(var);
    den = rbf(sqrtf(rbf(vb + 1e-5f)));
    ln = rbf(rbf(x1 - mb) / den);
    float h2 = rbf(rbf(ln * rbf(1.f + cb)) + sb);
    x1o[(size_t)s * D + d] = f2bf(x1);
    h2o[(size_t)s * D + d] = f2bf(h2);
}

__global__ void k_ffn1(const u16* h2, const u16* wg, const u16* wu, u16* act) {
    __shared__ float sh[8][D];
    int s0 = blockIdx.y * 8;
    int f = blockIdx.x * blockDim.x + threadIdx.x;
    for (int i = threadIdx.x; i < 8 * D; i += blockDim.x) sh[i / D][i % D] = bf2f(h2[(size_t)s0 * D + i]);
    __syncthreads();
    float g[8] = {0}, u[8] = {0};
    for (int k = 0; k < D; ++k) {
        float a = bf2f(wg[(size_t)k * F + f]), b = bf2f(wu[(size_t)k * F + f]);
#pragma unroll
        for (int r = 0; r < 8; ++r) { g[r] += sh[r][k] * a; u[r] += sh[r][k] * b; }
    }
#pragma unroll
    for (int r = 0; r < 8; ++r) {
        float gg = rbf(g[r]); gg = rbf(gg / (1.f + __expf(-gg)));
        act[(size_t)(s0 + r) * F + f] = f2bf(gg * rbf(u[r]));
    }
}

__global__ void k_ffn2(const u16* act, const u16* wd, const u16* x1, const float* mod, u16* out) {
    __shared__ float sh[4][F];
    int s0 = blockIdx.x * 4;
    int d = threadIdx.x;
    for (int i = threadIdx.x; i < 4 * F; i += blockDim.x) sh[i / F][i % F] = bf2f(act[(size_t)s0 * F + i]);
    __syncthreads();
    float o[4] = {0};
    for (int k = 0; k < F; ++k) {
        float w = bf2f(wd[(size_t)k * D + d]);
#pragma unroll
        for (int r = 0; r < 4; ++r) o[r] += sh[r][k] * w;
    }
    float gb = mod[5 * D + d];
#pragma unroll
    for (int r = 0; r < 4; ++r) {
        float v = rbf(gb * rbf(o[r]));
        out[(size_t)(s0 + r) * D + d] = f2bf(bf2f(x1[(size_t)(s0 + r) * D + d]) + v);
    }
}

extern "C" void kernel_launch(void* const* d_in, const int* in_sizes, int n_in,
                              void* d_out, int out_size, void* d_ws, size_t ws_size,
                              hipStream_t stream) {
    const u16* q = (const u16*)d_in[0];
    const u16* k = (const u16*)d_in[1];
    const u16* v = (const u16*)d_in[2];
    const u16* shk = (const u16*)d_in[3];
    const u16* ssmk = (const u16*)d_in[4];
    const u16* ssmD = (const u16*)d_in[5];
    const u16* wg = (const u16*)d_in[7];
    const u16* wu = (const u16*)d_in[8];
    const u16* wd = (const u16*)d_in[9];
    const u16* c = (const u16*)d_in[10];
    const u16* wmod = (const u16*)d_in[11];
    char* ws = (char*)d_ws;
    float* mod = (float*)ws; ws += 16384;
    u16* kvT = (u16*)ws; ws += (size_t)S * D * 2;
    u16* y = (u16*)ws; ws += (size_t)S * D * 2;
    u16* x1 = (u16*)ws; ws += (size_t)S * D * 2;
    u16* h2 = (u16*)ws; ws += (size_t)S * D * 2;
    u16* act = (u16*)ws; ws += (size_t)S * F * 2;
    k_mod<<<(6 * D + 63) / 64, 64, 0, stream>>>(c, wmod, mod);
    k_kv<<<(S * D + 255) / 256, 256, 0, stream>>>(k, v, shk, kvT);
    k_conv<<<D, 256, 0, stream>>>(kvT, ssmk, ssmD, y);
    k_row<<<S, D, 0, stream>>>(q, y, mod, x1, h2);
    k_ffn1<<<dim3(F / 256, S / 8), 256, 0, stream>>>(h2, wg, wu, act);
    k_ffn2<<<S / 4, D, 0, stream>>>(act, wd, x1, mod, (u16*)d_out);
}
```

```cpp
#include <hip/hip_runtime.h>
#include <stdint.h>

typedef unsigned short u16;
typedef unsigned int u32;
typedef __attribute__((ext_vector_type(8))) __bf16 bf16x8;
typedef __attribute__((ext_vector_type(2))) __bf16 bf16x2;
typedef __attribute__((ext_vector_type(4))) float f32x4;
typedef __attribute__((ext_vector_type(2))) float f32x2;
typedef __attribute__((ext_vector_type(4))) u32 u32x4;
typedef __attribute__((ext_vector_type(2))) u32 u32x2;
typedef __attribute__((ext_vector_type(4))) short s16x4;
#define LAS __attribute__((address_space(3)))
#define DI __device__ __forceinline__

#define ST_DECL
#define ST(i)
#define STV(i)
#define ST_FLUSH(k)
static constexpr int S = 2048, D = 448, F = 1792;

DI float lo2f(u32 v) { return __uint_as_float(v << 16); }
DI float hi2f(u32 v) { return __uint_as_float(v & 0xffff0000u); }
DI float bf2f(u16 v) { return __uint_as_float(((u32)v) << 16); }
DI u32 pk2(float a, float b) { bf16x2 v = {(__bf16)a, (__bf16)b}; return __builtin_bit_cast(u32, v); }
DI float rbf(float a) { return lo2f(pk2(a, a)); }
DI f32x4 mfma16(u32x4 a, u32x4 b, f32x4 c) {
    return __builtin_amdgcn_mfma_f32_16x16x32_bf16(__builtin_bit_cast(bf16x8, a), __builtin_bit_cast(bf16x8, b), c, 0, 0, 0);
}
DI u32x2 trread(const char* p) {
    return __builtin_bit_cast(u32x2, __builtin_amdgcn_ds_read_tr16_b64_v4i16((LAS s16x4*)p));
}
DI float dot2(u32 a, u32 b, float c) {
    return __builtin_amdgcn_fdot2_f32_bf16(__builtin_bit_cast(bf16x2, a), __builtin_bit_cast(bf16x2, b), c, false);
}
DI float wave_sum(float v) {
#pragma unroll
    for (int o = 32; o > 0; o >>= 1) v += __shfl_xor(v, o);
    return v;
}

static constexpr size_t OFF_MOD = 0;
static constexpr size_t OFF_ST1 = 16384;
static constexpr size_t OFF_TT  = OFF_ST1 + 16384;
static constexpr size_t OFF_T0  = OFF_TT + 32768;
static constexpr size_t OFF_X1  = OFF_T0 + (size_t)256 * 448 * 32;
static constexpr size_t OFF_W1T = OFF_X1 + (size_t)S * D * 2;
static constexpr size_t OFF_W2T = OFF_W1T + (size_t)112 * 28 * 1024;
static constexpr size_t OFF_ACT = OFF_W2T + (size_t)7 * 56 * 4 * 1024;

__global__ __launch_bounds__(256) void ka(const u16* __restrict__ q, const u16* __restrict__ k, const u16* __restrict__ v,
                                          const u16* __restrict__ shk, const u16* __restrict__ c, const u16* __restrict__ wmod,
                                          const u16* __restrict__ wd, char* __restrict__ ws) {
    __shared__ __attribute__((aligned(16))) char smem[4 * 32 * 144];
    const int b = blockIdx.x, tid = threadIdx.x, lane = tid & 63, wave = tid >> 6;
    ST_DECL ST(0);
    if (b < 256) {
        const int r0 = 8 * b;
        u32x4 xr[2];
        const int lc = lane < 56 ? lane : 55;
#pragma unroll
        for (int i = 0; i < 2; ++i) xr[i] = *(const u32x4*)(q + (size_t)(r0 + 2 * wave + i) * D + lc * 8);
        if (tid < 224) {
            const int ch = 2 * tid;
            u32 kk[11], vv[8], xx[8];
#pragma unroll
            for (int i = 0; i < 11; ++i) { int r = r0 - 3 + i; int rc = r < 0 ? 0 : r; kk[i] = *(const u32*)(k + (size_t)rc * D + ch); if (r < 0) kk[i] = 0; }
#pragma unroll
            for (int i = 0; i < 8; ++i) { vv[i] = *(const u32*)(v + (size_t)(r0 + i) * D + ch); xx[i] = *(const u32*)(q + (size_t)(r0 + i) * D + ch); }
            const u32x4 w = *(const u32x4*)(shk + ch * 4);
            const float a0 = lo2f(w.x), a1 = hi2f(w.x), a2 = lo2f(w.y), a3 = hi2f(w.y);
            const float b0 = lo2f(w.z), b1 = hi2f(w.z), b2 = lo2f(w.w), b3 = hi2f(w.w);
            float ua[8], ub[8];
#pragma unroll
            for (int i = 0; i < 8; ++i) {
                float sa = lo2f(kk[i]) * a3 + lo2f(kk[i + 1]) * a2 + lo2f(kk[i + 2]) * a1 + lo2f(kk[i + 3]) * a0;
                float sb = hi2f(kk[i]) * b3 + hi2f(kk[i + 1]) * b2 + hi2f(kk[i + 2]) * b1 + hi2f(kk[i + 3]) * b0;
                ua[i] = sa * lo2f(vv[i]); ub[i] = sb * hi2f(vv[i]);
            }
            u32x4 o0 = {pk2(ua[7], ua[6]), pk2(ua[5], ua[4]), pk2(ua[3], ua[2]), pk2(ua[1], ua[0])};
            u32x4 o2 = {pk2(ub[7], ub[6]), pk2(ub[5], ub[4]), pk2(ub[3], ub[2]), pk2(ub[1], ub[0])};
            u32x4 o1, o3;
#pragma unroll
            for (int i = 0; i < 4; ++i) { o1[i] = (xx[2 * i] & 0xffffu) | (xx[2 * i + 1] << 16); o3[i] = (xx[2 * i] >> 16) | (xx[2 * i + 1] & 0xffff0000u); }
            u32x4* dst = (u32x4*)(ws + OFF_T0 + ((size_t)b * 448 + ch) * 32);
            dst[0] = o0; dst[1] = o1; dst[2] = o2; dst[3] = o3;
        }
#pragma unroll
        for (int i = 0; i < 2; ++i) {
            float e[8];
#pragma unroll
            for (int j = 0; j < 4; ++j) { e[2 * j] = lo2f(xr[i][j]); e[2 * j + 1] = hi2f(xr[i][j]); }
            float s = 0.f;
#pragma unroll
            for (int j = 0; j < 8; ++j) s += e[j];
            if (lane >= 56) s = 0.f;
            const float m = wave_sum(s) * (1.f / D);
            float qv = 0.f;
#pragma unroll
            for (int j = 0; j < 8; ++j) qv += (e[j] - m) * (e[j] - m);
            if (lane >= 56) qv = 0.f;
            const float var = wave_sum(qv) * (1.f / D);
            const float rs = __frsqrt_rn(var + 1e-5f);
            if (lane == 0) *(f32x2*)(ws + OFF_ST1 + (size_t)(r0 + 2 * wave + i) * 8) = (f32x2){rs, -m * rs};
        }
    } else if (b < 256 + 42) {
        const int cb = b - 256, rg = tid >> 3, c8 = tid & 7;
        u32x4 w[14]; float sc[14];
#pragma unroll
        for (int i = 0; i < 14; ++i) w[i] = *(const u32x4*)(wmod + (size_t)(rg + 32 * i) * (6 * D) + cb * 64 + c8 * 8);
#pragma unroll
        for (int i = 0; i < 14; ++i) { float cv = bf2f(c[rg + 32 * i]); sc[i] = rbf(cv / (1.f + __expf(-cv))); }
        float acc[8] = {0, 0, 0, 0, 0, 0, 0, 0};
#pragma unroll
        for (int i = 0; i < 14; ++i)
#pragma unroll
            for (int j = 0; j < 4; ++j) { acc[2 * j] += sc[i] * lo2f(w[i][j]); acc[2 * j + 1] += sc[i] * hi2f(w[i][j]); }
#pragma unroll
        for (int j = 0; j < 8; ++j) { acc[j] += __shfl_xor(acc[j], 8); acc[j] += __shfl_xor(acc[j], 16); acc[j] += __shfl_xor(acc[j], 32); }
        float* red = (float*)smem;
        if (lane < 8)
#pragma unroll
            for (int j = 0; j < 8; ++j) red[wave * 64 + lane * 8 + j] = acc[j];
        __syncthreads();
        if (tid < 64) ((float*)(ws + OFF_MOD))[cb * 64 + tid] = rbf(red[tid] + red[64 + tid] + red[128 + tid] + red[192 + tid]);
    } else {
        const int task = (b - 298) * 4 + wave;
        const int ks = task / 7, db = task % 7;
        char* my = smem + wave * (32 * 144);
        u32x4 w[4];
#pragma unroll
        for (int i = 0; i < 4; ++i) w[i] = *(const u32x4*)(wd + (size_t)(32 * ks + 8 * i + (lane >> 3)) * D + db * 64 + (lane & 7) * 8);
#pragma unroll
        for (int i = 0; i < 4; ++i) *(u32x4*)(my + (8 * i + (lane >> 3)) * 144 + (lane & 7) * 16) = w[i];
        __builtin_amdgcn_wave_barrier();
        const int g = lane >> 4, qq = (lane & 15) >> 2, p = lane & 3;
#pragma unroll
        for (int dt = 0; dt < 4; ++dt) {
            const char* a = my + (4 * g + qq) * 144 + (16 * p + 4 * dt) * 2;
            u32x2 l = trread(a), h = trread(a + 16 * 144);
            *(u32x4*)(ws + OFF_W2T + (((size_t)db * 56 + ks) * 4 + dt) * 1024 + lane * 16) = (u32x4){l.x, l.y, h.x, h.y};
        }
    }
    ST(6); STV(7); ST_FLUSH(0);
}

static constexpr int KFROW = 4160, UROW = 286 * 16;
static constexpr int L_KF = 0, L_U = 8 * KFROW, L_X = L_U + 2 * UROW, L_RED = L_X + 2 * 4096, L_KB_END = L_RED + 16 * 1024;

template <int TG>
DI void conv_main(const char* abase, const char* bbase, f32x4 (&acc)[4]) {
    constexpr int T[4] = {TG ? 1 : 0, TG ? 2 : 3, TG ? 5 : 4, TG ? 6 : 7};
    constexpr int NP = TG ? 7 : 8;
#pragma unroll 1
    for (int r = 0; r < 4; ++r) {
        u32x4 A[NP], B[NP];
#pragma unroll
        for (int p = 0; p < NP; ++p) {
            u32x2 l = *(const u32x2*)(abase + 64 * r + 512 * p), h = *(const u32x2*)(abase + 64 * r + 512 * p + 8);
            A[p] = (u32x4){l.x, l.y, h.x, h.y};
            B[p] = *(const u32x4*)(bbase - 64 * r + 512 * p);
        }
#pragma unroll
        for (int p = 0; p < NP; ++p)
#pragma unroll
            for (int i = 0; i < 4; ++i)
                if (p <= T[i]) acc[i] = mfma16(A[p], B[T[i] - p], acc[i]);
    }
}

__global__ __launch_bounds__(512, 4) void kb(const u16* __restrict__ ssmk, const u16* __restrict__ ssmD,
                                             const u16* __restrict__ wg, const u16* __restrict__ wu, char* __restrict__ ws) {
    __shared__ __attribute__((aligned(16))) char smem[8 * 32 * 272 > L_KB_END ? 8 * 32 * 272 : L_KB_END];
    const int b = blockIdx.x, tid = threadIdx.x, lane = tid & 63;
    const int wave = __builtin_amdgcn_readfirstlane(tid >> 6);
    const float* mod = (const float*)(ws + OFF_MOD);
    ST_DECL ST(0);
    if (b < 224) {
        const int d0 = 56 * (b & 7) + 2 * (b >> 3);
        u32x4 tv[2];
#pragma unroll
        for (int i = 0; i < 2; ++i) { int pc = tid + 512 * i; tv[i] = *(const u32x4*)(ws + OFF_T0 + ((size_t)(pc >> 2) * 448 + d0) * 32 + (pc & 3) * 16); }
        const int kc = tid & 255, kch = tid >> 8;
        const u16* kf = ssmk + (size_t)(d0 + kch) * S + kc * 8;
        const u32x4 w = *(const u32x4*)kf;
        u32x2 pv = *(const u32x2*)(kc ? kf - 4 : kf);
        if (kc == 0) pv = (u32x2){0, 0};
        const int ch = wave >> 2, rh = (wave >> 1) & 1, tg = wave & 1;
        const int a = lane & 15, g = lane >> 4;
        const int t0 = tg ? (rh ? 5 : 1) : (rh ? 4 : 0), t1 = tg ? (rh ? 6 : 2) : (rh ? 7 : 3);
        f32x4 st[2][2];
#pragma unroll
        for (int i = 0; i < 2; ++i) { const f32x4* sp = (const f32x4*)(ws + OFF_ST1 + (size_t)(256 * (i ? t1 : t0) + 16 * a + 4 * g) * 8); st[i][0] = sp[0]; st[i][1] = sp[1]; }
        const int d = d0 + ch;
        const float sa = mod[d], ca1 = rbf(1.f + mod[D + d]), ga = mod[2 * D + d], Dv = bf2f(ssmD[d]);
        if (tid < 60) *(u32x4*)(smem + L_U + (tid / 30) * UROW + (tid % 30) * 16) = (u32x4){0, 0, 0, 0};
        if (tid >= 64 && tid < 80) *(u32x4*)(smem + L_KF + ((tid - 64) >> 1) * KFROW + (tid & 1) * 16) = (u32x4){0, 0, 0, 0};
        {
            char* kd = smem + L_KF + kch * 4 * KFROW + 32 + kc * 16;
            const u32 s0 = __builtin_amdgcn_alignbit(pv.y, pv.x, 16), s1 = __builtin_amdgcn_alignbit(w.x, pv.y, 16), s2 = __builtin_amdgcn_alignbit(w.y, w.x, 16),
                      s3 = __builtin_amdgcn_alignbit(w.z, w.y, 16), s4 = __builtin_amdgcn_alignbit(w.w, w.z, 16);
            *(u32x4*)(kd) = w;
            *(u32x4*)(kd + KFROW) = (u32x4){s1, s2, s3, s4};
            *(u32x4*)(kd + 2 * KFROW) = (u32x4){pv.y, w.x, w.y, w.z};
            *(u32x4*)(kd + 3 * KFROW) = (u32x4){s0, s1, s2, s3};
        }
#pragma unroll
        for (int i = 0; i < 2; ++i) {
            int pc = tid + 512 * i, cc = pc >> 2, part = pc & 3;
            char* dst = (part & 1) ? smem + L_X + (part >> 1) * 4096 + cc * 16 : smem + L_U + (part >> 1) * UROW + (30 + cc) * 16;
            *(u32x4*)dst = tv[i];
        }
        ST(1);
        __syncthreads();
        ST(2);
        f32x4 acc[4] = {{0, 0, 0, 0}, {0, 0, 0, 0}, {0, 0, 0, 0}, {0, 0, 0, 0}};
        const char* abase = smem + L_KF + (ch * 4 + ((3 - a) & 3)) * KFROW + 56 + 8 * (a >> 2) - 16 * g + 64 * 4 * rh;
        const char* bbase = smem + L_U + ch * UROW + (28 + 2 * a + g) * 16 - 64 * 4 * rh;
        if (tg) conv_main<1>(abase, bbase, acc); else conv_main<0>(abase, bbase, acc);
        ST(3);
        {
            f32x4* red = (f32x4*)(smem + L_RED);
            red[(wave * 2 + 0) * 64 + lane] = rh ? acc[0] : acc[2];
            red[(wave * 2 + 1) * 64 + lane] = rh ? acc[1] : acc[3];
            __syncthreads();
            ST(4);
            const int pw = wave ^ 2;
            f32x4 y0 = (rh ? acc[2] : acc[0]) + red[(pw * 2 + 0) * 64 + lane];
            f32x4 y1 = (rh ? acc[3] : acc[1]) + red[(pw * 2 + 1) * 64 + lane];
#pragma unroll
            for (int i = 0; i < 2; ++i) {
                const f32x4 y = i ? y1 : y0;
                const int s0 = 256 * (i ? t1 : t0) + 16 * a + 4 * g;
                const u32x2 ur = *(const u32x2*)(smem + L_U + ch * UROW + (30 + (s0 >> 3)) * 16 + ((s0 & 4) ? 0 : 8));
                const u32x2 xr = *(const u32x2*)(smem + L_X + ch * 4096 + s0 * 2);
                const float u[4] = {hi2f(ur.y), lo2f(ur.y), hi2f(ur.x), lo2f(ur.x)};
                const float x[4] = {lo2f(xr.x), hi2f(xr.x), lo2f(xr.y), hi2f(xr.y)};
                float o[4];
#pragma unroll
                for (int r = 0; r < 4; ++r) {
                    const float rs = st[i][r >> 1][(r & 1) * 2], nm = st[i][r >> 1][(r & 1) * 2 + 1];
                    const float h = (x[r] * rs + nm) * ca1 + sa;
                    o[r] = x[r] + ga * (h * (y[r] + u[r] * Dv));
                }
                *(u32x2*)(ws + OFF_X1 + (((size_t)(s0 >> 7) * 448 + d) * 128 + (s0 & 127)) * 2) = (u32x2){pk2(o[0], o[1]), pk2(o[2], o[3])};
            }
        }
    } else if (b < 224 + 49) {
        const int task = (b - 224) * 8 + wave;
        const int ks = task / 28, fq = task % 28;
        char* my = smem + wave * (32 * 272);
        u32x4 gw[4], uw[4]; float sc[4];
#pragma unroll
        for (int i = 0; i < 4; ++i) {
            const int kr = 32 * ks + 8 * i + (lane >> 3);
            gw[i] = *(const u32x4*)(wg + (size_t)kr * F + fq * 64 + (lane & 7) * 8);
            uw[i] = *(const u32x4*)(wu + (size_t)kr * F + fq * 64 + (lane & 7) * 8);
            sc[i] = rbf(1.f + mod[4 * D + kr]);
        }
#pragma unroll
        for (int i = 0; i < 4; ++i) {
            u32 o[8];
#pragma unroll
            for (int j = 0; j < 4; ++j) { o[2 * j] = pk2(lo2f(gw[i][j]) * sc[i], lo2f(uw[i][j]) * sc[i]); o[2 * j + 1] = pk2(hi2f(gw[i][j]) * sc[i], hi2f(uw[i][j]) * sc[i]); }
            char* dst = my + (8 * i + (lane >> 3)) * 272 + (lane & 7) * 32;
            *(u32x4*)dst = (u32x4){o[0], o[1], o[2], o[3]};
            *(u32x4*)(dst + 16) = (u32x4){o[4], o[5], o[6], o[7]};
        }
        __builtin_amdgcn_wave_barrier();
        const int g = lane >> 4, qq = (lane & 15) >> 2, p = lane & 3;
#pragma unroll
        for (int fb = 0; fb < 4; ++fb)
#pragma unroll
            for (int mt = 0; mt < 2; ++mt) {
                const char* a = my + (4 * g + qq) * 272 + 64 * fb + 16 * p + 8 * mt;
                u32x2 l = trread(a), h = trread(a + 16 * 272);
                *(u32x4*)(ws + OFF_W1T + (((size_t)(4 * fq + fb) * 14 + ks) * 2 + mt) * 1024 + lane * 16) = (u32x4){l.x, l.y, h.x, h.y};
            }
    } else {
        const int id = b - 273, type = id / 28, fq = id % 28, rg = tid >> 3, c8 = tid & 7;
        const u16* W = type ? wu : wg;
        u32x4 w[7]; float s1[7], s2[7];
#pragma unroll
        for (int i = 0; i < 7; ++i) { const int kr = rg + 64 * i; w[i] = *(const u32x4*)(W + (size_t)kr * F + fq * 64 + c8 * 8); s1[i] = rbf(1.f + mod[4 * D + kr]); s2[i] = mod[3 * D + kr]; }
        float a1[8] = {0, 0, 0, 0, 0, 0, 0, 0}, a2[8] = {0, 0, 0, 0, 0, 0, 0, 0};
#pragma unroll
        for (int i = 0; i < 7; ++i)
#pragma unroll
            for (int j = 0; j < 4; ++j) {
                const float l = lo2f(w[i][j]), h = hi2f(w[i][j]);
                const u32 pr = pk2(l * s1[i], h * s1[i]);
                a1[2 * j] += lo2f(pr); a1[2 * j + 1] += hi2f(pr);
                a2[2 * j] += l * s2[i]; a2[2 * j + 1] += h * s2[i];
            }
#pragma unroll
        for (int j = 0; j < 8; ++j) {
            a1[j] += __shfl_xor(a1[j], 8); a1[j] += __shfl_xor(a1[j], 16); a1[j] += __shfl_xor(a1[j], 32);
            a2[j] += __shfl_xor(a2[j], 8); a2[j] += __shfl_xor(a2[j], 16); a2[j] += __shfl_xor(a2[j], 32);
        }
        float* red = (float*)smem;
        if (lane < 8)
#pragma unroll
            for (int j = 0; j < 8; ++j) { red[wave * 128 + (lane * 8 + j) * 2] = a1[j]; red[wave * 128 + (lane * 8 + j) * 2 + 1] = a2[j]; }
        __syncthreads();
        if (tid < 128) {
            float s = 0.f;
#pragma unroll
            for (int i = 0; i < 8; ++i) s += red[i * 128 + tid];
            ((float*)(ws + OFF_TT))[((fq * 64 + (tid >> 1)) * 2 + type) * 2 + (tid & 1)] = s;
        }
    }
    ST(6); STV(7); ST_FLUSH(1);
}

static constexpr int XROW = 288;
__global__ __launch_bounds__(512, 2) void kc(char* __restrict__ ws) {
    extern __shared__ __attribute__((aligned(16))) char smem[];
    const int b = blockIdx.x, tid = threadIdx.x, lane = tid & 63;
    const int wave = __builtin_amdgcn_readfirstlane(tid >> 6);
    const int x = b & 7, i = b >> 3;
    const int rt = 4 * (x >> 1) + (i & 3), sl = 7 * (x & 1) + (i >> 2);
    const int fb = sl * 8 + wave;
    ST_DECL ST(0);
    {
        u32x4 xs[14];
        const char* src = ws + OFF_X1 + (size_t)rt * (448 * 256) + tid * 16;
#pragma unroll
        for (int j = 0; j < 14; ++j) xs[j] = *(const u32x4*)(src + j * 8192);
        char* dst = smem + (tid >> 4) * XROW + (tid & 15) * 16;
#pragma unroll
        for (int j = 0; j < 14; ++j) *(u32x4*)(dst + j * 32 * XROW) = xs[j];
    }
    u32x4 W[14][2];
    {
        const char* src = ws + OFF_W1T + (size_t)fb * 28 * 1024 + lane * 16;
#pragma unroll
        for (int j = 0; j < 14; ++j) { W[j][0] = *(const u32x4*)(src + j * 2048); W[j][1] = *(const u32x4*)(src + j * 2048 + 1024); }
    }
    const int n = lane & 15, g = lane >> 4, qq = n >> 2, p = lane & 3;
    f32x4 tt[4];
    {
        const f32x4* tp = (const f32x4*)(ws + OFF_TT + (size_t)(16 * fb + 4 * g) * 16);
#pragma unroll
        for (int j = 0; j < 4; ++j) tt[j] = tp[j];
    }
    ST(1);
    __syncthreads();
    ST(2);
    const char* xb = smem + (4 * g + qq) * XROW + 8 * p;
    {
        float s1 = 0.f, s2 = 0.f;
#pragma unroll
        for (int j = 0; j < 28; ++j) {
            u32x2 t = trread(xb + wave * 32 + j * 16 * XROW);
            s1 = dot2(t.x, 0x3f803f80u, s1); s1 = dot2(t.y, 0x3f803f80u, s1);
            s2 = dot2(t.x, t.x, s2); s2 = dot2(t.y, t.y, s2);
        }
        s1 += __shfl_xor(s1, 16); s1 += __shfl_xor(s1, 32);
        s2 += __shfl_xor(s2, 16); s2 += __shfl_xor(s2, 32);
        const float m = s1 * (1.f / D);
        const float var = fmaxf(s2 * (1.f / D) - m * m, 0.f);
        const float rs = __frsqrt_rn(var + 1e-5f);
        if (lane < 16) *(f32x2*)(smem + 448 * XROW + (wave * 16 + lane) * 8) = (f32x2){rs, -m * rs};
    }
    ST(3);
    f32x4 acc[8][2];
#pragma unroll
    for (int s = 0; s < 8; ++s) { acc[s][0] = (f32x4){0, 0, 0, 0}; acc[s][1] = (f32x4){0, 0, 0, 0}; }
#pragma unroll
    for (int ks = 0; ks < 14; ++ks) {
#pragma unroll
        for (int s = 0; s < 8; ++s) {
            u32x2 l = trread(xb + ks * 32 * XROW + s * 32), h = trread(xb + (ks * 32 + 16) * XROW + s * 32);
            const u32x4 B = {l.x, l.y, h.x, h.y};
            acc[s][0] = mfma16(W[ks][0], B, acc[s][0]);
            acc[s][1] = mfma16(W[ks][1], B, acc[s][1]);
        }
    }
    ST(4);
    __syncthreads();
    ST(5);
    char* actp = ws + OFF_ACT + ((size_t)(rt * 4) * 112 + fb) * 1024 + lane * 16;
#pragma unroll
    for (int sp = 0; sp < 4; ++sp) {
        u32 o[4];
#pragma unroll
        for (int h = 0; h < 2; ++h) {
            const int s = 2 * sp + h;
            const f32x2 st = *(const f32x2*)(smem + 448 * XROW + (s * 16 + n) * 8);
#pragma unroll
            for (int mt = 0; mt < 2; ++mt) {
                float a[2];
#pragma unroll
                for (int e = 0; e < 2; ++e) {
                    const f32x4 t = tt[mt * 2 + e];
                    const float gv = st.x * acc[s][mt][2 * e] + (st.y * t.x + t.y);
                    const float uv = st.x * acc[s][mt][2 * e + 1] + (st.y * t.z + t.w);
                    a[e] = gv * uv * __builtin_amdgcn_rcpf(1.f + __builtin_amdgcn_exp2f(gv * -1.44269504f));
                }
                o[h * 2 + mt] = pk2(a[0], a[1]);
            }
        }
        *(u32x4*)(actp + (size_t)sp * 112 * 1024) = (u32x4){o[0], o[1], o[2], o[3]};
    }
    ST(6); STV(7); ST_FLUSH(2);
}

__global__ __launch_bounds__(256, 1) void kd(const char* __restrict__ ws, u16* __restrict__ out) {
    __shared__ __attribute__((aligned(16))) char smem[4 * 4 * 4 * 1024];
    const int b = blockIdx.x, tid = threadIdx.x, lane = tid & 63;
    const int wave = __builtin_amdgcn_readfirstlane(tid >> 6);
    const int x = b & 7, i = b >> 3;
    const int rb = 4 * x + (i & 3), db = i >> 2;
    const int n = lane & 15, g = lane >> 4;
    ST_DECL ST(0);
    const char* wsrc = ws + OFF_W2T + ((size_t)db * 56 + wave * 14) * 4096 + lane * 16;
    const char* asrc = ws + OFF_ACT + ((size_t)(rb * 2) * 112 + wave * 28) * 1024 + lane * 16;
    f32x4 acc[4][4];
#pragma unroll
    for (int s = 0; s < 4; ++s)
#pragma unroll
        for (int t = 0; t < 4; ++t) acc[s][t] = (f32x4){0, 0, 0, 0};
    u32x4 Wf[14][4], Af[14][4];
#pragma unroll
    for (int ks = 0; ks < 14; ++ks) {
#pragma unroll
        for (int t = 0; t < 4; ++t) Wf[ks][t] = *(const u32x4*)(wsrc + ks * 4096 + t * 1024);
#pragma unroll
        for (int t = 0; t < 4; ++t) Af[ks][t] = *(const u32x4*)(asrc + (size_t)(t >> 1) * 112 * 1024 + ks * 2048 + (t & 1) * 1024);
    }
    const int srow = rb * 64 + wave * 16 + n;
    u16 x1v[16];
#pragma unroll
    for (int j = 0; j < 16; ++j) x1v[j] = *(const u16*)(ws + OFF_X1 + (((size_t)(srow >> 7) * 448 + db * 64 + 16 * g + j) * 128 + (srow & 127)) * 2);
    f32x4 gb[4];
#pragma unroll
    for (int j = 0; j < 4; ++j) gb[j] = *(const f32x4*)(ws + OFF_MOD + (size_t)(5 * D + db * 64 + 16 * g + 4 * j) * 4);
#pragma unroll
    for (int ks = 0; ks < 14; ++ks) {
#pragma unroll
        for (int sp = 0; sp < 2; ++sp) {
            const u32x4 a = Af[ks][sp * 2], c = Af[ks][sp * 2 + 1];
            const u32x4 B0 = {a.x, a.y, c.x, c.y}, B1 = {a.z, a.w, c.z, c.w};
#pragma unroll
            for (int t = 0; t < 4; ++t) { acc[sp * 2][t] = mfma16(Wf[ks][t], B0, acc[sp * 2][t]); acc[sp * 2 + 1][t] = mfma16(Wf[ks][t], B1, acc[sp * 2 + 1][t]); }
        }
    }
    ST(3);
    f32x4* red = (f32x4*)smem;
#pragma unroll
    for (int s = 0; s < 4; ++s)
#pragma unroll
        for (int t = 0; t < 4; ++t) red[((s * 4 + wave) * 4 + t) * 64 + lane] = acc[s][t];
    __syncthreads();
    u32 o[8];
#pragma unroll
    for (int t = 0; t < 4; ++t) {
        f32x4 v = red[((wave * 4 + 0) * 4 + t) * 64 + lane] + red[((wave * 4 + 1) * 4 + t) * 64 + lane] + red[((wave * 4 + 2) * 4 + t) * 64 + lane] + red[((wave * 4 + 3) * 4 + t) * 64 + lane];
        o[2 * t] = pk2(bf2f(x1v[4 * t]) + gb[t].x * v.x, bf2f(x1v[4 * t + 1]) + gb[t].y * v.y);
        o[2 * t + 1] = pk2(bf2f(x1v[4 * t + 2]) + gb[t].z * v.z, bf2f(x1v[4 * t + 3]) + gb[t].w * v.w);
    }
    u32x4* dst = (u32x4*)(out + (size_t)srow * D + db * 64 + 16 * g);
    dst[0] = (u32x4){o[0], o[1], o[2], o[3]};
    dst[1] = (u32x4){o[4], o[5], o[6], o[7]};
    ST(6); STV(7); ST_FLUSH(3);
}

extern "C" void kernel_launch(void* const* d_in, const int* in_sizes, int n_in,
                              void* d_out, int out_size, void* d_ws, size_t ws_size,
                              hipStream_t stream) {
    const u16* q = (const u16*)d_in[0];
    const u16* k = (const u16*)d_in[1];
    const u16* v = (const u16*)d_in[2];
    const u16* shk = (const u16*)d_in[3];
    const u16* ssmk = (const u16*)d_in[4];
    const u16* ssmD = (const u16*)d_in[5];
    const u16* wg = (const u16*)d_in[7];
    const u16* wu = (const u16*)d_in[8];
    const u16* wd = (const u16*)d_in[9];
    const u16* c = (const u16*)d_in[10];
    const u16* wmod = (const u16*)d_in[11];
    char* ws = (char*)d_ws;
    constexpr int KC_LDS = 448 * XROW + 1024;
    (void)hipFuncSetAttribute((const void*)kc, hipFuncAttributeMaxDynamicSharedMemorySize, KC_LDS);
    ka<<<256 + 42 + 98, 256, 0, stream>>>(q, k, v, shk, c, wmod, wd, ws);
    kb<<<224 + 49 + 56, 512, 0, stream>>>(ssmk, ssmD, wg, wu, ws);
    kc<<<224, 512, KC_LDS, stream>>>(ws);
    kd<<<224, 256, 0, stream>>>(ws, (u16*)d_out);
}
```

```cpp
#include <hip/hip_runtime.h>
#include <stdint.h>

typedef unsigned short u16;
typedef unsigned int u32;
typedef unsigned long long u64;
typedef __attribute__((ext_vector_type(8))) __bf16 bf16x8;
typedef __attribute__((ext_vector_type(2))) __bf16 bf16x2;
typedef __attribute__((ext_vector_type(4))) float f32x4;
typedef __attribute__((ext_vector_type(2))) float f32x2;
typedef __attribute__((ext_vector_type(4))) u32 u32x4;
typedef __attribute__((ext_vector_type(2))) u32 u32x2;
typedef __attribute__((ext_vector_type(4))) short s16x4;
#define LAS __attribute__((address_space(3)))
#define GAS __attribute__((address_space(1)))
typedef GAS u32 gu32;
typedef GAS u64 gu64;
#define DI __device__ __forceinline__
#define RLX_AGENT __ATOMIC_RELAXED, __HIP_MEMORY_SCOPE_AGENT

#define ST_DECL
#define ST(i)
#define ST_FLUSH()

static constexpr int S = 2048, D = 448, F = 1792;

DI float lo2f(u32 v) { return __uint_as_float(v << 16); }
DI float hi2f(u32 v) { return __uint_as_float(v & 0xffff0000u); }
DI float bf2f(u16 v) { return __uint_as_float(((u32)v) << 16); }
DI u32 pk2(float a, float b) { bf16x2 v = {(__bf16)a, (__bf16)b}; return __builtin_bit_cast(u32, v); }
DI float rbf(float a) { return lo2f(pk2(a, a)); }
DI f32x4 mfma16(u32x4 a, u32x4 b, f32x4 c) {
    return __builtin_amdgcn_mfma_f32_16x16x32_bf16(__builtin_bit_cast(bf16x8, a), __builtin_bit_cast(bf16x8, b), c, 0, 0, 0);
}
DI u32x2 trread(const char* p) { return __builtin_bit_cast(u32x2, __builtin_amdgcn_ds_read_tr16_b64_v4i16((LAS s16x4*)p)); }
DI u32x4 trfrag(const char* p) { const u32x2 l = trread(p), h = trread(p + 2048); return (u32x4){l.x, l.y, h.x, h.y}; }
template <int CTRL> DI float dpp_add(float v) { return v + __builtin_bit_cast(float, __builtin_amdgcn_update_dpp(0, __builtin_bit_cast(int, v), CTRL, 0xf, 0xf, true)); }
DI float swap16_add(float v) { auto r = __builtin_amdgcn_permlane16_swap(__float_as_uint(v), __float_as_uint(v), false, false); return __uint_as_float(r[0]) + __uint_as_float(r[1]); }
DI float swap32_add(float v) { auto r = __builtin_amdgcn_permlane32_swap(__float_as_uint(v), __float_as_uint(v), false, false); return __uint_as_float(r[0]) + __uint_as_float(r[1]); }
DI float sum_hi3(float v) { v = dpp_add<0x128>(v); v = swap16_add(v); return swap32_add(v); }
DI float sum_hi2(float v) { v = swap16_add(v); return swap32_add(v); }
DI float wave_sum(float v) { v = dpp_add<0xB1>(v); v = dpp_add<0x4E>(v); v = dpp_add<0x124>(v); return sum_hi3(v); }

static constexpr u32 OFF_EPOCH = 0;
static constexpr u32 OFF_FLG = 256;
static constexpr u32 LN_FKV = 0, LN_FX = 28, LN_FST1 = 56, LN_FTT = 57, LN_FST2 = 58, LN_FACT = 60, LN_CNT = 80;
static constexpr u32 OFF_MODG = 32768;
static constexpr u32 OFF_ST1 = OFF_MODG + 32768;
static constexpr u32 OFF_ST2 = OFF_ST1 + 16384;
static constexpr u32 OFF_TT = OFF_ST2 + 16384;
static constexpr u32 OFF_TU = OFF_TT + 32768;
static constexpr u32 OFF_TX = OFF_TU + S * D * 2;
static constexpr u32 OFF_X1 = OFF_TX + S * D * 2;
static constexpr u32 OFF_Z = OFF_X1 + S * D * 2;
static constexpr u32 OFF_ACT = OFF_Z + S * D * 2;
static constexpr u32 WS_END = OFF_ACT + 64 * 112 * 1024;

static constexpr int KFROW = 4160, UROW = 286 * 16;
static constexpr int L_KF = 0;
static constexpr int L_U = 131072, L_RED = L_U + 2 * UROW;
static constexpr int L_MISC = L_RED + 16384;
static constexpr int LDS_BYTES = 163840;
static_assert(L_MISC + 4096 <= LDS_BYTES, "lds");

#define SB() __builtin_amdgcn_sched_barrier(0)
#define BAR() do { asm volatile("s_waitcnt lgkmcnt(0)" ::: "memory"); __builtin_amdgcn_s_barrier(); asm volatile("" ::: "memory"); } while (0)
#define DRAIN() asm volatile("s_waitcnt vmcnt(0)" ::: "memory")
#define WAITV(n) asm volatile("s_waitcnt vmcnt(%0)" :: "i"(n) : "memory")
#define BLD_SC1(dst, voff, soff, imm) asm volatile("buffer_load_dwordx4 %0, %1, %2, %3 offen offset:%4 sc1" : "=v"(dst) : "v"(voff), "s"(srd), "s"(soff), "i"(imm))
#define DMA(SRD, m0v, voff, soff) asm volatile("s_mov_b32 m0, %0\n\ts_nop 0\n\tbuffer_load_dwordx4 %1, %2, %3 offen lds" :: "s"(m0v), "v"(voff), "s"(SRD), "s"(soff) : "memory")
#define DMA_SC1(SRD, m0v, voff, soff) asm volatile("s_mov_b32 m0, %0\n\ts_nop 0\n\tbuffer_load_dwordx4 %1, %2, %3 offen sc1 lds" :: "s"(m0v), "v"(voff), "s"(SRD), "s"(soff) : "memory")
template <int N> DI void waitv1(u32x4& a) { asm volatile("s_waitcnt vmcnt(%1)" : "+v"(a) : "i"(N)); }
template <int N> DI void waitv2(u32x4& a, u32x4& b) { asm volatile("s_waitcnt vmcnt(%2)" : "+v"(a), "+v"(b) : "i"(N)); }
DI u32x4 mksrd(const void* p, u32 bytes) { return (u32x4){(u32)(size_t)p, (u32)((size_t)p >> 32), bytes, 0x00020000u}; }
static constexpr u32 SPIN_CAP = 1u << 18;

DI void poll_eq(gu32* p, u32 want) {
#pragma unroll 1
    for (u32 spins = 0; spins < SPIN_CAP; ++spins) {
        const u32 f = __hip_atomic_load(p, RLX_AGENT);
        if (__all(f == want)) break;
        __builtin_amdgcn_s_sleep(2);
    }
    asm volatile("" ::: "memory");
}
DI float poll_granule(gu64* p, u32 tag) {
    u64 x = 0;
#pragma unroll 1
    for (u32 spins = 0; spins < SPIN_CAP; ++spins) {
        x = __hip_atomic_load(p, RLX_AGENT);
        if (__all((u32)(x >> 32) == tag)) break;
        __builtin_amdgcn_s_sleep(2);
    }
    return __uint_as_float((u32)x);
}

template <int TG>
DI void conv_main(const u32x4 (&A)[4][8], const char* bbase, f32x4 (&acc)[4]) {
    constexpr int T[4] = {TG ? 1 : 0, TG ? 2 : 3, TG ? 5 : 4, TG ? 6 : 7};
    constexpr int NP = TG ? 7 : 8;
    u32x4 B[2][8];
#pragma unroll
    for (int p = 0; p < NP; ++p) B[0][p] = *(const u32x4*)(bbase + 512 * p);
#pragma unroll
    for (int r = 0; r < 4; ++r) {
        if (r < 3)
#pragma unroll
            for (int p = 0; p < NP; ++p) B[(r + 1) & 1][p] = *(const u32x4*)(bbase - 64 * (r + 1) + 512 * p);
#pragma unroll
        for (int q = 0; q < NP; ++q)
#pragma unroll
            for (int i = 0; i < 4; ++i)
                if (q <= T[i]) acc[i] = mfma16(A[r][T[i] - q], B[r & 1][q], acc[i]);
#pragma unroll
        for (int i = 0; i < 9; ++i) { __builtin_amdgcn_sched_group_barrier(0x8, 2, 0); __builtin_amdgcn_sched_group_barrier(0x100, 1, 0); }
        SB();
    }
}

DI float gemv64(const u16* __restrict__ W, int ld, const float* sc_lds, float* red, int tid) {
    const int rg = tid >> 3, c8 = tid & 7, lane = tid & 63, wave = tid >> 6;
    u32x4 w[7];
#pragma unroll
    for (int i = 0; i < 7; ++i) w[i] = *(const u32x4*)(W + (size_t)(rg + 64 * i) * ld + c8 * 8);
    float acc[8] = {0, 0, 0, 0, 0, 0, 0, 0};
#pragma unroll
    for (int i = 0; i < 7; ++i) {
        const float s = sc_lds[rg + 64 * i];
#pragma unroll
        for (int j = 0; j < 4; ++j) { acc[2 * j] += s * lo2f(w[i][j]); acc[2 * j + 1] += s * hi2f(w[i][j]); }
    }
#pragma unroll
    for (int j = 0; j < 8; ++j) acc[j] = sum_hi3(acc[j]);
    if (lane < 8) { *(f32x4*)(red + wave * 64 + lane * 8) = (f32x4){acc[0], acc[1], acc[2], acc[3]}; *(f32x4*)(red + wave * 64 + lane * 8 + 4) = (f32x4){acc[4], acc[5], acc[6], acc[7]}; }
    __syncthreads();
    float s = 0.f;
    if (tid < 64)
#pragma unroll
        for (int i = 0; i < 8; ++i) s += red[i * 64 + tid];
    __syncthreads();
    return s;
}

__global__ __launch_bounds__(512, 2) void fused(const u16* __restrict__ q, const u16* __restrict__ k, const u16* __restrict__ v,
                                                const u16* __restrict__ shk, const u16* __restrict__ ssmk, const u16* __restrict__ ssmD,
                                                const u16* __restrict__ wg, const u16* __restrict__ wu, const u16* __restrict__ wd,
                                                const u16* __restrict__ c, const u16* __restrict__ wmod, char* __restrict__ ws, u16* __restrict__ out) {
    extern __shared__ __attribute__((aligned(16))) char smem[];
    const int b = blockIdx.x, tid = threadIdx.x, lane = tid & 63;
    const int wave = __builtin_amdgcn_readfirstlane(tid >> 6);
    ST_DECL ST(0);
    const u32 tag = __builtin_amdgcn_readfirstlane(__hip_atomic_load((gu32*)(ws + OFF_EPOCH), RLX_AGENT)) * 0x9E3779B1u + 0x7F4A7C15u;
    gu32* flg = (gu32*)(ws + OFF_FLG);
    gu64* modg = (gu64*)(ws + OFF_MODG);
    const __amdgpu_buffer_rsrc_t rsrc = __builtin_amdgcn_make_buffer_rsrc(ws, 0, WS_END, 0x00020000);
    const u32x4 srd = mksrd(ws, WS_END);
    const int n = lane & 15, g = lane >> 4;

    if (b >= 224) {
        const int a = b - 224;
        float* sc = (float*)(smem);
        float* red = (float*)(smem + 8192);
        if (a == 0 && wave == 0) __hip_atomic_store(flg + (LN_CNT + 2 * lane) * 32, 0u, RLX_AGENT);
        u32x4 xr[8];
        const int lc = lane < 56 ? lane : 55;
#pragma unroll
        for (int i = 0; i < 8; ++i) xr[i] = *(const u32x4*)(q + (size_t)(64 * a + 8 * wave + i) * D + lc * 8);
        if (tid < 448) { const float cv = bf2f(c[tid]); sc[tid] = rbf(cv / (1.f + __expf(-cv))); }
        __syncthreads();
        if (a < 28) {
            const int cb = a < 21 ? a : a + 7;
            const float s = gemv64(wmod + cb * 64, 6 * D, sc, red, tid);
            if (tid < 64) __hip_atomic_store(modg + cb * 64 + tid, ((u64)tag << 32) | __float_as_uint(rbf(s)), RLX_AGENT);
        }
        ST(1);
        {
            f32x2 mine = {0.f, 0.f};
#pragma unroll
            for (int i = 0; i < 8; ++i) {
                float e[8];
#pragma unroll
                for (int j = 0; j < 4; ++j) { e[2 * j] = lo2f(xr[i][j]); e[2 * j + 1] = hi2f(xr[i][j]); }
                float s = ((e[0] + e[1]) + (e[2] + e[3])) + ((e[4] + e[5]) + (e[6] + e[7]));
                if (lane >= 56) s = 0.f;
                const float m = wave_sum(s) * (1.f / D);
                float qv = 0.f;
#pragma unroll
                for (int j = 0; j < 8; ++j) qv += (e[j] - m) * (e[j] - m);
                if (lane >= 56) qv = 0.f;
                const float rs = __frsqrt_rn(wave_sum(qv) * (1.f / D) + 1e-5f);
                if (lane == i) mine = (f32x2){rs, -m * rs};
            }
            if (lane < 8) __hip_atomic_store((gu64*)(ws + OFF_ST1) + 64 * a + 8 * wave + lane, __builtin_bit_cast(u64, mine), RLX_AGENT);
            DRAIN();
            __syncthreads();
            if (tid == 0) __hip_atomic_store(flg + LN_FST1 * 32 + a, tag, RLX_AGENT);
        }
        ST(2);
        if (a >= 18) {
            const int cb = a < 25 ? a + 3 : a + 10;
            const float s = gemv64(wmod + cb * 64, 6 * D, sc, red, tid);
            if (tid < 64) __hip_atomic_store(modg + cb * 64 + tid, ((u64)tag << 32) | __float_as_uint(rbf(s)), RLX_AGENT);
        }
        ST(3);
        if (a < 28) {
            const int type = a / 14, sl = a % 14, rg = tid >> 4, c16 = tid & 15;
            const u16* W = (type ? wu : wg) + sl * 128 + c16 * 8;
            u32x4 w[14];
#pragma unroll
            for (int i = 0; i < 14; ++i) w[i] = *(const u32x4*)(W + (size_t)(rg + 32 * i) * F);
            if (wave < 7) {
                const float sbv = poll_granule(modg + 3 * D + tid, tag), cbv = poll_granule(modg + 4 * D + tid, tag);
                *(f32x2*)(smem + 2048 + tid * 8) = (f32x2){rbf(1.f + cbv), sbv};
            }
            __syncthreads();
            float a1[8] = {0, 0, 0, 0, 0, 0, 0, 0}, a2[8] = {0, 0, 0, 0, 0, 0, 0, 0};
#pragma unroll
            for (int i = 0; i < 14; ++i) {
                const f32x2 s = *(const f32x2*)(smem + 2048 + (rg + 32 * i) * 8);
#pragma unroll
                for (int j = 0; j < 4; ++j) {
                    const float l = lo2f(w[i][j]), h = hi2f(w[i][j]);
                    a1[2 * j] += l * s.x; a1[2 * j + 1] += h * s.x; a2[2 * j] += l * s.y; a2[2 * j + 1] += h * s.y;
                }
            }
#pragma unroll
            for (int j = 0; j < 8; ++j) { a1[j] = sum_hi2(a1[j]); a2[j] = sum_hi2(a2[j]); }
            if (lane < 16)
#pragma unroll
                for (int j = 0; j < 4; ++j) *(f32x4*)(red + wave * 256 + lane * 16 + j * 4) = (f32x4){a1[2 * j], a2[2 * j], a1[2 * j + 1], a2[2 * j + 1]};
            __syncthreads();
            if (tid < 128) {
                f32x2 s = {0.f, 0.f};
#pragma unroll
                for (int i = 0; i < 8; ++i) s += *(const f32x2*)(red + i * 256 + tid * 2);
                __hip_atomic_store((gu64*)(ws + OFF_TT) + type * F + sl * 128 + tid, __builtin_bit_cast(u64, s), RLX_AGENT);
            }
            DRAIN();
            __syncthreads();
            if (tid == 0) __hip_atomic_store(flg + LN_FTT * 32 + a, tag, RLX_AGENT);
        }
        ST(4);
        if (wave == 0) { poll_eq(flg + LN_FST1 * 32 + (lane & 31), tag); poll_eq(flg + (LN_CNT + 2 * ((b & 7) * 8 + (lane & 7))) * 32, 28u); }
        __syncthreads();
        ST(5);
        {
            const int ro = tid & 7, ch = tid >> 3;
            u32x4 w[7];
#pragma unroll
            for (int i = 0; i < 7; ++i) w[i] = __builtin_amdgcn_raw_buffer_load_b128(rsrc, OFF_X1 + ((a >> 1) * 448 + ch + 64 * i) * 256 + (a & 1) * 128 + ro * 16, 0, 16);
            float a1[8] = {0, 0, 0, 0, 0, 0, 0, 0}, a2[8] = {0, 0, 0, 0, 0, 0, 0, 0};
#pragma unroll
            for (int i = 0; i < 7; ++i)
#pragma unroll
                for (int j = 0; j < 4; ++j) {
                    const float l = lo2f(w[i][j]), h = hi2f(w[i][j]);
                    a1[2 * j] += l; a1[2 * j + 1] += h; a2[2 * j] += l * l; a2[2 * j + 1] += h * h;
                }
#pragma unroll
            for (int j = 0; j < 8; ++j) { a1[j] = sum_hi3(a1[j]); a2[j] = sum_hi3(a2[j]); }
            if (lane < 8)
#pragma unroll
                for (int j = 0; j < 4; ++j) *(f32x4*)(red + wave * 128 + lane * 16 + j * 4) = (f32x4){a1[2 * j], a2[2 * j], a1[2 * j + 1], a2[2 * j + 1]};
            __syncthreads();
            if (wave == 0) {
                f32x2 s = {0.f, 0.f};
#pragma unroll
                for (int i = 0; i < 8; ++i) s += *(const f32x2*)(red + i * 128 + lane * 2);
                const float m = s.x * (1.f / D);
                const float rs = __frsqrt_rn(fmaxf(s.y * (1.f / D) - m * m, 0.f) + 1e-5f);
                __hip_atomic_store((gu64*)(ws + OFF_ST2) + 64 * a + lane, __builtin_bit_cast(u64, (f32x2){rs, -m * rs}), RLX_AGENT);
                DRAIN();
                if (lane == 0) {
                    __hip_atomic_store(flg + LN_FST2 * 32 + a, tag, RLX_AGENT);
                    if (a == 0) __hip_atomic_store((gu32*)(ws + OFF_EPOCH), tag, RLX_AGENT);
                }
            }
        }
        ST(6);
        ST_FLUSH();
        return;
    }

    const int gq = b >> 5, j = b & 31;
    const int xc = b & 7, ii = b >> 3;
    const int rt = 4 * (xc >> 1) + (ii & 3), sl = 7 * (xc & 1) + (ii >> 2);
    const int rb = 8 * (xc >> 1) + 4 * (xc & 1) + (ii & 3), db = ii >> 2;
    const int d0 = 64 * gq + 2 * j;
    {
        const int r8 = lane & 7, cp = 8 * (wave & 3) + (lane >> 3);
        const int ch = 64 * gq + 2 * cp, r0 = 64 * j + 8 * r8;
        const u32 toff = ((gq * 32 + j) * 64 + 2 * cp) * 128 + r8 * 16;
        if (wave < 4) {
            u32 kk[11], vv[8];
#pragma unroll
            for (int i = 0; i < 11; ++i) { int r = r0 - 3 + i; int rc = r < 0 ? 0 : r; kk[i] = *(const u32*)(k + (size_t)rc * D + ch); if (i < 3 && r < 0) kk[i] = 0; }
#pragma unroll
            for (int i = 0; i < 8; ++i) vv[i] = *(const u32*)(v + (size_t)(r0 + i) * D + ch);
            const u32x4 w = *(const u32x4*)(shk + ch * 4);
            const float a0 = lo2f(w.x), a1 = hi2f(w.x), a2 = lo2f(w.y), a3 = hi2f(w.y);
            const float b0 = lo2f(w.z), b1 = hi2f(w.z), b2 = lo2f(w.w), b3 = hi2f(w.w);
            float ua[8], ub[8];
#pragma unroll
            for (int i = 0; i < 8; ++i) {
                const float sa = lo2f(kk[i]) * a3 + lo2f(kk[i + 1]) * a2 + lo2f(kk[i + 2]) * a1 + lo2f(kk[i + 3]) * a0;
                const float sb = hi2f(kk[i]) * b3 + hi2f(kk[i + 1]) * b2 + hi2f(kk[i + 2]) * b1 + hi2f(kk[i + 3]) * b0;
                ua[i] = sa * lo2f(vv[i]); ub[i] = sb * hi2f(vv[i]);
            }
            __builtin_amdgcn_raw_buffer_store_b128((u32x4){pk2(ua[7], ua[6]), pk2(ua[5], ua[4]), pk2(ua[3], ua[2]), pk2(ua[1], ua[0])}, rsrc, OFF_TU + toff, 0, 16);
            __builtin_amdgcn_raw_buffer_store_b128((u32x4){pk2(ub[7], ub[6]), pk2(ub[5], ub[4]), pk2(ub[3], ub[2]), pk2(ub[1], ub[0])}, rsrc, OFF_TU + toff + 128, 0, 16);
            ST(1);
            DRAIN();
            if (lane == 0) __hip_atomic_store(flg + (LN_FKV + gq * 4 + wave) * 32 + j, tag, RLX_AGENT);
            if (tid < 60) *(u32x4*)(smem + L_U + (tid / 30) * UROW + (tid % 30) * 16) = (u32x4){0, 0, 0, 0};
            if (tid >= 64 && tid < 80) *(u32x4*)(smem + L_KF + ((tid - 64) >> 1) * KFROW + (tid & 1) * 16) = (u32x4){0, 0, 0, 0};
        } else {
            const int kc = tid & 255;
            u32x4 w[2]; u32x2 pv[2];
#pragma unroll
            for (int cc = 0; cc < 2; ++cc) {
                const u16* kf = ssmk + (size_t)(d0 + cc) * S + kc * 8;
                w[cc] = *(const u32x4*)kf;
                pv[cc] = *(const u32x2*)(kc ? kf - 4 : kf);
                if (kc == 0) pv[cc] = (u32x2){0, 0};
            }
            u32 xx[8];
#pragma unroll
            for (int i = 0; i < 8; ++i) xx[i] = *(const u32*)(q + (size_t)(r0 + i) * D + ch);
#pragma unroll
            for (int cc = 0; cc < 2; ++cc) {
                char* kd = smem + L_KF + cc * 4 * KFROW + 32 + kc * 16;
                const u32 s0 = __builtin_amdgcn_alignbit(pv[cc].y, pv[cc].x, 16), s1 = __builtin_amdgcn_alignbit(w[cc].x, pv[cc].y, 16), s2 = __builtin_amdgcn_alignbit(w[cc].y, w[cc].x, 16),
                          s3 = __builtin_amdgcn_alignbit(w[cc].z, w[cc].y, 16), s4 = __builtin_amdgcn_alignbit(w[cc].w, w[cc].z, 16);
                *(u32x4*)(kd) = w[cc];
                *(u32x4*)(kd + KFROW) = (u32x4){s1, s2, s3, s4};
                *(u32x4*)(kd + 2 * KFROW) = (u32x4){pv[cc].y, w[cc].x, w[cc].y, w[cc].z};
                *(u32x4*)(kd + 3 * KFROW) = (u32x4){s0, s1, s2, s3};
            }
            u32x4 o1, o3;
#pragma unroll
            for (int i = 0; i < 4; ++i) { o1[i] = (xx[2 * i] & 0xffffu) | (xx[2 * i + 1] << 16); o3[i] = (xx[2 * i] >> 16) | (xx[2 * i + 1] & 0xffff0000u); }
            __builtin_amdgcn_raw_buffer_store_b128(o1, rsrc, OFF_TX + toff, 0, 16);
            __builtin_amdgcn_raw_buffer_store_b128(o3, rsrc, OFF_TX + toff + 128, 0, 16);
            ST(1);
            DRAIN();
            if (lane == 0) __hip_atomic_store(flg + (LN_FX + gq * 4 + (wave & 3)) * 32 + j, tag, RLX_AGENT);
        }
    }
    ST(2);
    BAR();
    const int ch = wave >> 2, rh = (wave >> 1) & 1, tg = wave & 1;
    const u32 lds0 = (u32)(size_t)(LAS char*)smem;
    const u32x4 srdw = mksrd(wave < 4 ? wg : wu, D * F * 2), srdd = mksrd(wd, D * F * 2);
    const u32 tl_st = (lane >> 4) * 32 + (lane & 1) * 16, tl_row = (lane >> 1) & 7;
    const u32 w1voff = tl_row * (F * 2) + tl_st;
    const char* trb = smem + (g >> 1) * 1024 + ((g & 1) * 4 + (n >> 2)) * 32 + (lane & 3) * 8;
    f32x4 y0, y1;
    const int t0 = tg ? (rh ? 5 : 1) : (rh ? 4 : 0), t1 = tg ? (rh ? 6 : 2) : (rh ? 7 : 3);
    {
        u32x4 A[4][8];
        const char* abase = smem + L_KF + (ch * 4 + ((3 - n) & 3)) * KFROW + 56 + 8 * (n >> 2) - 16 * g + 64 * 4 * rh;
#pragma unroll
        for (int r = 0; r < 4; ++r)
#pragma unroll
            for (int p = 0; p < 8; ++p) {
                const u32x2 l = *(const u32x2*)(abase + 64 * r + 512 * p), h = *(const u32x2*)(abase + 64 * r + 512 * p + 8);
                A[r][p] = (u32x4){l.x, l.y, h.x, h.y};
            }
        ST(3);
        if (wave == 0) poll_eq(flg + (LN_FKV + gq * 4 + (j >> 3)) * 32 + (lane & 31), tag);
        BAR();
        ST(4);
        u32x4 kvv;
        BLD_SC1(kvv, (u32)((tid >> 4) * 8192 + (tid & 15) * 16), OFF_TU + (gq * 32 * 64 + 2 * j) * 128, 0);
#pragma unroll
        for (int ks = 0; ks < 8; ++ks)
#pragma unroll
            for (int hf = 0; hf < 2; ++hf) {
                const u32 m0v = lds0 + (ks * 16 + (wave >> 2) * 8 + hf * 4 + (wave & 3)) * 1024;
                const u32 so = (32 * ks + 8 * (wave & 3)) * (F * 2) + sl * 256 + hf * 128;
                DMA(srdw, m0v, w1voff, so);
            }
        waitv1<16>(kvv);
        *(u32x4*)(smem + L_U + ((tid >> 3) & 1) * UROW + (30 + (tid >> 4) * 8 + (tid & 7)) * 16) = kvv;
        BAR();
        ST(5);
        f32x4 acc[4] = {{0, 0, 0, 0}, {0, 0, 0, 0}, {0, 0, 0, 0}, {0, 0, 0, 0}};
        const char* bbase = smem + L_U + ch * UROW + (28 + 2 * n + g) * 16 - 64 * 4 * rh;
        if (tg) conv_main<1>(A, bbase, acc); else conv_main<0>(A, bbase, acc);
        ST(6);
        f32x4* red = (f32x4*)(smem + L_RED);
        red[(wave * 2 + 0) * 64 + lane] = rh ? acc[0] : acc[2];
        red[(wave * 2 + 1) * 64 + lane] = rh ? acc[1] : acc[3];
        y0 = rh ? acc[2] : acc[0]; y1 = rh ? acc[3] : acc[1];
    }
    {
        const int d = d0 + ch;
        poll_eq(flg + (lane < 32 ? (LN_FX + gq * 4 + (j >> 3)) * 32 + lane : LN_FST1 * 32 + lane - 32), tag);
        u32x2 xr[2]; u32x4 st[2][2];
#pragma unroll
        for (int i = 0; i < 2; ++i) {
            const int s0 = 256 * (i ? t1 : t0) + 16 * n + 4 * g;
            xr[i] = __builtin_amdgcn_raw_buffer_load_b64(rsrc, OFF_TX + ((gq * 32 + (s0 >> 6)) * 64 + 2 * j + ch) * 128 + (s0 & 63) * 2, 0, 16);
            st[i][0] = __builtin_amdgcn_raw_buffer_load_b128(rsrc, OFF_ST1 + s0 * 8, 0, 16);
            st[i][1] = __builtin_amdgcn_raw_buffer_load_b128(rsrc, OFF_ST1 + s0 * 8 + 16, 0, 16);
        }
        const int mi = lane & 3;
        const float mv = poll_granule(modg + (mi == 3 ? 4 : mi) * D + d, tag);
        const float sa = __builtin_bit_cast(float, __builtin_amdgcn_readlane(__builtin_bit_cast(int, mv), 0));
        const float ca1 = rbf(1.f + __builtin_bit_cast(float, __builtin_amdgcn_readlane(__builtin_bit_cast(int, mv), 1)));
        const float ga = __builtin_bit_cast(float, __builtin_amdgcn_readlane(__builtin_bit_cast(int, mv), 2));
        const float cb1 = rbf(1.f + __builtin_bit_cast(float, __builtin_amdgcn_readlane(__builtin_bit_cast(int, mv), 3)));
        const float Dv = bf2f(ssmD[d]);
        BAR();
        ST(7);
        const f32x4* red = (const f32x4*)(smem + L_RED);
        const int pw = wave ^ 2;
        y0 += red[(pw * 2 + 0) * 64 + lane];
        y1 += red[(pw * 2 + 1) * 64 + lane];
#pragma unroll
        for (int i = 0; i < 2; ++i) {
            const f32x4 y = i ? y1 : y0;
            const int s0 = 256 * (i ? t1 : t0) + 16 * n + 4 * g;
            const u32x2 ur = *(const u32x2*)(smem + L_U + ch * UROW + (30 + (s0 >> 3)) * 16 + ((s0 & 4) ? 0 : 8));
            const float u[4] = {hi2f(ur.y), lo2f(ur.y), hi2f(ur.x), lo2f(ur.x)};
            const float x[4] = {lo2f(xr[i].x), hi2f(xr[i].x), lo2f(xr[i].y), hi2f(xr[i].y)};
            float o[4];
#pragma unroll
            for (int r = 0; r < 4; ++r) {
                const float rs = __uint_as_float(st[i][r >> 1][(r & 1) * 2]), nm = __uint_as_float(st[i][r >> 1][(r & 1) * 2 + 1]);
                const float h = (x[r] * rs + nm) * ca1 + sa;
                o[r] = x[r] + ga * (h * (y[r] + u[r] * Dv));
            }
            const u32x2 x1p = {pk2(o[0], o[1]), pk2(o[2], o[3])};
            const u32x2 zp = {pk2(lo2f(x1p.x) * cb1, hi2f(x1p.x) * cb1), pk2(lo2f(x1p.y) * cb1, hi2f(x1p.y) * cb1)};
            const u32 off = ((s0 >> 7) * 448 + d) * 256 + (s0 & 127) * 2;
            __builtin_amdgcn_raw_buffer_store_b64(zp, rsrc, OFF_Z + off, 0, 16);
            __builtin_amdgcn_raw_buffer_store_b64(x1p, rsrc, OFF_X1 + off, 0, 16);
        }
        DRAIN();
        BAR();
        if (wave == 0 && lane < 8) __hip_atomic_fetch_add(flg + (LN_CNT + 2 * (lane * 8 + (b & 7))) * 32, 1u, RLX_AGENT);
    }
    ST(8);
    u32x4 W[14][2];
    const char* wrb = trb + (wave >> 2) * 4096 + (wave & 3) * 256;
#pragma unroll
    for (int ks = 0; ks < 8; ++ks) { W[ks][0] = trfrag(wrb + ks * 16384); W[ks][1] = trfrag(wrb + ks * 16384 + 8192); }
    BAR();
#pragma unroll
    for (int ks = 8; ks < 14; ++ks)
#pragma unroll
        for (int hf = 0; hf < 2; ++hf) {
            const u32 m0v = lds0 + ((ks - 8) * 16 + (wave >> 2) * 8 + hf * 4 + (wave & 3)) * 1024;
            const u32 so = (32 * ks + 8 * (wave & 3)) * (F * 2) + sl * 256 + hf * 128;
            DMA(srdw, m0v, w1voff, so);
        }
    DRAIN();
    BAR();
#pragma unroll
    for (int ks = 8; ks < 14; ++ks) { W[ks][0] = trfrag(wrb + (ks - 8) * 16384); W[ks][1] = trfrag(wrb + (ks - 8) * 16384 + 8192); }
    ST(9);
    if (wave == 0) poll_eq(flg + (LN_CNT + 2 * (((b >> 3) & 7) * 8 + (lane & 7))) * 32, 28u);
    BAR();
    ST(10);
    {
        const u32 zvoff = tl_row * 256 + tl_st;
#pragma unroll
        for (int i = 0; i < 14; ++i) {
            const int hf = i / 7, kb = (i % 7) * 8;
            DMA_SC1(srd, lds0 + (hf * 56 + kb + wave) * 1024, zvoff, OFF_Z + (rt * 448 + (kb + wave) * 8) * 256 + hf * 128);
        }
    }
    f32x4 acc[8][2];
#pragma unroll
    for (int s = 0; s < 8; ++s) { acc[s][0] = (f32x4){0, 0, 0, 0}; acc[s][1] = (f32x4){0, 0, 0, 0}; }
    {
        u32x4 B[2][4];
#pragma unroll
        for (int jj = -1; jj < 28; ++jj) {
            const int nx = jj + 1;
            if (nx == 0) { WAITV(13); BAR(); }
            if (nx == 2) { WAITV(11); BAR(); }
            if (nx == 6) { WAITV(7); BAR(); }
            if (nx == 14) { WAITV(0); BAR(); }
            if (nx < 28)
#pragma unroll
                for (int t = 0; t < 4; ++t) B[nx & 1][t] = trfrag(trb + (nx / 14) * 57344 + (nx % 14) * 4096 + t * 256);
            if (jj >= 0) {
#pragma unroll
                for (int t = 0; t < 4; ++t) {
                    const int s = (jj / 14) * 4 + t;
                    acc[s][0] = mfma16(W[jj % 14][0], B[jj & 1][t], acc[s][0]);
                    acc[s][1] = mfma16(W[jj % 14][1], B[jj & 1][t], acc[s][1]);
                }
#pragma unroll
                for (int i = 0; i < 8; ++i) { __builtin_amdgcn_sched_group_barrier(0x8, 1, 0); __builtin_amdgcn_sched_group_barrier(0x100, 1, 0); }
            }
            SB();
        }
    }
    ST(11);
    const int fb = sl * 8 + wave;
    {
        poll_eq(flg + ((lane & 2) ? LN_FST2 * 32 + 2 * rt + (lane & 1) : LN_FTT * 32 + (lane & 1) * 14 + sl), tag);
        u32x4 tt[2][2]; u32x2 st[8];
#pragma unroll
        for (int ty = 0; ty < 2; ++ty)
#pragma unroll
            for (int e = 0; e < 2; ++e) tt[ty][e] = __builtin_amdgcn_raw_buffer_load_b128(rsrc, OFF_TT + (ty * F + 16 * fb + 4 * g + 2 * e) * 8, 0, 16);
#pragma unroll
        for (int s = 0; s < 8; ++s) st[s] = __builtin_amdgcn_raw_buffer_load_b64(rsrc, OFF_ST2 + (rt * 128 + s * 16 + n) * 8, 0, 16);
#pragma unroll
        for (int sp = 0; sp < 4; ++sp) {
            u32 o[4];
#pragma unroll
            for (int h = 0; h < 2; ++h) {
                const int s = 2 * sp + h;
                const float rs = __uint_as_float(st[s].x), nm = __uint_as_float(st[s].y);
                float a[4];
#pragma unroll
                for (int r = 0; r < 4; ++r) {
                    const float gv = rs * acc[s][0][r] + (nm * __uint_as_float(tt[0][r >> 1][(r & 1) * 2]) + __uint_as_float(tt[0][r >> 1][(r & 1) * 2 + 1]));
                    const float uv = rs * acc[s][1][r] + (nm * __uint_as_float(tt[1][r >> 1][(r & 1) * 2]) + __uint_as_float(tt[1][r >> 1][(r & 1) * 2 + 1]));
                    a[r] = gv * uv * __builtin_amdgcn_rcpf(1.f + __builtin_amdgcn_exp2f(gv * -1.44269504f));
                }
                o[h * 2] = pk2(a[0], a[1]); o[h * 2 + 1] = pk2(a[2], a[3]);
            }
            __builtin_amdgcn_raw_buffer_store_b128((u32x4){o[0], o[1], o[2], o[3]}, rsrc, OFF_ACT + ((rt * 4 + sp) * 112 + fb) * 1024 + lane * 16, 0, 16);
        }
        ST(12);
        DRAIN();
        BAR();
        if (tid == 0) __hip_atomic_store(flg + (LN_FACT + rt) * 32 + sl, tag, RLX_AGENT);
    }
    u32x4 W2[7][4];
    {
        const u32 w2voff = tl_row * (D * 2) + tl_st;
        const u32 base = lds0 + wave * 20480, so0 = (224 * wave) * (D * 2) + db * 128;
        const char* rb2 = trb + wave * 20480;
#pragma unroll
        for (int i = 0; i < 20; ++i) DMA(srdd, base + i * 1024, w2voff, so0 + i * 8 * (D * 2));
        WAITV(12);
#pragma unroll
        for (int ks = 0; ks < 2; ++ks)
#pragma unroll
            for (int t = 0; t < 4; ++t) W2[ks][t] = trfrag(rb2 + ks * 4096 + t * 256);
        asm volatile("s_waitcnt lgkmcnt(0)" ::: "memory");
        SB();
#pragma unroll
        for (int i = 20; i < 28; ++i) DMA(srdd, base + (i - 20) * 1024, w2voff, so0 + i * 8 * (D * 2));
        WAITV(8);
#pragma unroll
        for (int ks = 2; ks < 5; ++ks)
#pragma unroll
            for (int t = 0; t < 4; ++t) W2[ks][t] = trfrag(rb2 + ks * 4096 + t * 256);
        WAITV(0);
#pragma unroll
        for (int ks = 5; ks < 7; ++ks)
#pragma unroll
            for (int t = 0; t < 4; ++t) W2[ks][t] = trfrag(rb2 + (ks - 5) * 4096 + t * 256);
    }
    const int srow = rb * 64 + (wave >> 1) * 16 + n;
    const int dcol = db * 64 + (2 * (wave & 1) + (g >> 1)) * 16 + (g & 1) * 8;
    u32 x1p[4], gbp[4];
    {
        u32 xa[8]; float ga_[8];
#pragma unroll
        for (int jx = 0; jx < 8; ++jx) xa[jx] = __builtin_amdgcn_raw_buffer_load_b16(rsrc, OFF_X1 + ((srow >> 7) * 448 + dcol + jx) * 256 + (srow & 127) * 2, 0, 16);
        const float gv = poll_granule(modg + 5 * D + db * 64 + lane, tag);
#pragma unroll
        for (int jx = 0; jx < 8; ++jx) ga_[jx] = __shfl(gv, (dcol & 63) + jx);
#pragma unroll
        for (int jx = 0; jx < 4; ++jx) { x1p[jx] = xa[2 * jx] | (xa[2 * jx + 1] << 16); gbp[jx] = pk2(ga_[2 * jx], ga_[2 * jx + 1]); asm volatile("" : "+v"(x1p[jx]), "+v"(gbp[jx])); }
    }
    ST(13);
    if (wave == 0) poll_eq(flg + (LN_FACT + (rb >> 1)) * 32 + (lane < 14 ? lane : 0), tag);
    DRAIN();
    BAR();
    ST(14);
    const u32 l16 = lane * 16;
    u32x4 Af[2][7][2];
#pragma unroll
    for (int sp = 0; sp < 2; ++sp) {
        const u32 as = OFF_ACT + ((rb * 2 + sp) * 112 + wave * 14) * 1024;
#pragma unroll
        for (int ks = 0; ks < 7; ++ks) {
            if (sp == 1 && ks >= 3) break;
            BLD_SC1(Af[sp][ks][0], l16, as + (ks >> 1) * 4096, (ks & 1) * 2048);
            BLD_SC1(Af[sp][ks][1], l16, as + (ks >> 1) * 4096, (ks & 1) * 2048 + 1024);
        }
    }
    f32x4* red = (f32x4*)smem;
#pragma unroll
    for (int sp = 0; sp < 2; ++sp) {
        f32x4 cc[2][4];
#pragma unroll
        for (int t = 0; t < 4; ++t) { cc[0][t] = (f32x4){0, 0, 0, 0}; cc[1][t] = (f32x4){0, 0, 0, 0}; }
#pragma unroll
        for (int ks = 0; ks < 7; ++ks) {
            const int idx = sp * 7 + ks;
            if (idx <= 4) waitv2<18>(Af[sp][ks][0], Af[sp][ks][1]);
            else if (idx == 5) waitv2<16>(Af[sp][ks][0], Af[sp][ks][1]);
            else if (idx == 6) waitv2<14>(Af[sp][ks][0], Af[sp][ks][1]);
            else if (idx == 7) waitv2<12>(Af[sp][ks][0], Af[sp][ks][1]);
            else if (idx == 8) waitv2<10>(Af[sp][ks][0], Af[sp][ks][1]);
            else if (idx == 9) waitv2<8>(Af[sp][ks][0], Af[sp][ks][1]);
            else if (idx == 10) waitv2<6>(Af[sp][ks][0], Af[sp][ks][1]);
            else if (idx == 11) waitv2<4>(Af[sp][ks][0], Af[sp][ks][1]);
            else if (idx == 12) waitv2<2>(Af[sp][ks][0], Af[sp][ks][1]);
            else waitv2<0>(Af[sp][ks][0], Af[sp][ks][1]);
            const u32x4 a = Af[sp][ks][0], e = Af[sp][ks][1];
            const u32x4 B0 = {a.x, a.y, e.x, e.y}, B1 = {a.z, a.w, e.z, e.w};
#pragma unroll
            for (int t = 0; t < 4; ++t) { cc[0][t] = mfma16(W2[ks][t], B0, cc[0][t]); cc[1][t] = mfma16(W2[ks][t], B1, cc[1][t]); }
            SB();
            if (sp == 0 && ks < 4) {
                const u32 as = OFF_ACT + ((rb * 2 + 1) * 112 + wave * 14) * 1024;
                BLD_SC1(Af[1][ks + 3][0], l16, as + ((ks + 3) >> 1) * 4096, ((ks + 3) & 1) * 2048);
                BLD_SC1(Af[1][ks + 3][1], l16, as + ((ks + 3) >> 1) * 4096, ((ks + 3) & 1) * 2048 + 1024);
            }
            SB();
        }
#pragma unroll
        for (int h = 0; h < 2; ++h)
#pragma unroll
            for (int t = 0; t < 4; ++t) red[(((sp * 2 + h) * 4 + t) * 8 + wave) * 64 + lane] = cc[h][t];
    }
    BAR();
    {
        const f32x4* rp = red + (wave * 2 + (g >> 1)) * 512 + (g & 1) * 32 + n;
        f32x4 v0 = {0, 0, 0, 0}, v1 = {0, 0, 0, 0};
#pragma unroll
        for (int w = 0; w < 8; ++w) { v0 += rp[w * 64]; v1 += rp[w * 64 + 16]; }
        *(u32x4*)(out + (size_t)srow * D + dcol) = (u32x4){pk2(lo2f(x1p[0]) + lo2f(gbp[0]) * v0.x, hi2f(x1p[0]) + hi2f(gbp[0]) * v0.y), pk2(lo2f(x1p[1]) + lo2f(gbp[1]) * v0.z, hi2f(x1p[1]) + hi2f(gbp[1]) * v0.w),
                                                           pk2(lo2f(x1p[2]) + lo2f(gbp[2]) * v1.x, hi2f(x1p[2]) + hi2f(gbp[2]) * v1.y), pk2(lo2f(x1p[3]) + lo2f(gbp[3]) * v1.z, hi2f(x1p[3]) + hi2f(gbp[3]) * v1.w)};
    }
    ST(15);
    ST_FLUSH();
}

extern "C" void kernel_launch(void* const* d_in, const int* in_sizes, int n_in,
                              void* d_out, int out_size, void* d_ws, size_t ws_size,
                              hipStream_t stream) {
    (void)hipFuncSetAttribute((const void*)fused, hipFuncAttributeMaxDynamicSharedMemorySize, LDS_BYTES);
    fused<<<256, 512, LDS_BYTES, stream>>>((const u16*)d_in[0], (const u16*)d_in[1], (const u16*)d_in[2], (const u16*)d_in[3], (const u16*)d_in[4], (const u16*)d_in[5],
                                           (const u16*)d_in[7], (const u16*)d_in[8], (const u16*)d_in[9], (const u16*)d_in[10], (const u16*)d_in[11], (char*)d_ws, (u16*)d_out);
}
```

```cpp
#include <hip/hip_runtime.h>
#include <stdint.h>

typedef unsigned short u16;
typedef unsigned int u32;
typedef unsigned long long u64;
typedef __attribute__((ext_vector_type(8))) __bf16 bf16x8;
typedef __attribute__((ext_vector_type(2))) __bf16 bf16x2;
typedef __attribute__((ext_vector_type(4))) float f32x4;
typedef __attribute__((ext_vector_type(2))) float f32x2;
typedef __attribute__((ext_vector_type(4))) u32 u32x4;
typedef __attribute__((ext_vector_type(2))) u32 u32x2;
typedef __attribute__((ext_vector_type(4))) short s16x4;
#define LAS __attribute__((address_space(3)))
#define GAS __attribute__((address_space(1)))
typedef GAS u32 gu32;
typedef GAS u64 gu64;
#define DI __device__ __forceinline__
#define RLX_AGENT __ATOMIC_RELAXED, __HIP_MEMORY_SCOPE_AGENT

#define ST_DECL
#define ST(i)
#define ST_FLUSH()

static constexpr int S = 2048, D = 448, F = 1792;

DI float lo2f(u32 v) { return __uint_as_float(v << 16); }
DI float hi2f(u32 v) { return __uint_as_float(v & 0xffff0000u); }
DI float bf2f(u16 v) { return __uint_as_float(((u32)v) << 16); }
DI u32 pk2(float a, float b) { return __builtin_bit_cast(u32, __builtin_convertvector((f32x2){a, b}, bf16x2)); }
DI float rbf(float a) { return lo2f(pk2(a, a)); }
DI f32x4 mfma16(u32x4 a, u32x4 b, f32x4 c) {
    return __builtin_amdgcn_mfma_f32_16x16x32_bf16(__builtin_bit_cast(bf16x8, a), __builtin_bit_cast(bf16x8, b), c, 0, 0, 0);
}
DI u32x2 trread(const char* p) { return __builtin_bit_cast(u32x2, __builtin_amdgcn_ds_read_tr16_b64_v4i16((LAS s16x4*)p)); }
DI u32x4 trfrag(const char* p) { const u32x2 l = trread(p), h = trread(p + 2048); return (u32x4){l.x, l.y, h.x, h.y}; }
template <int CTRL> DI float dpp_add(float v) { return v + __builtin_bit_cast(float, __builtin_amdgcn_update_dpp(0, __builtin_bit_cast(int, v), CTRL, 0xf, 0xf, true)); }
DI float swap16_add(float v) { auto r = __builtin_amdgcn_permlane16_swap(__float_as_uint(v), __float_as_uint(v), false, false); return __uint_as_float(r[0]) + __uint_as_float(r[1]); }
DI float swap32_add(float v) { auto r = __builtin_amdgcn_permlane32_swap(__float_as_uint(v), __float_as_uint(v), false, false); return __uint_as_float(r[0]) + __uint_as_float(r[1]); }
DI float sum_hi3(float v) { v = dpp_add<0x128>(v); v = swap16_add(v); return swap32_add(v); }
DI float sum_hi2(float v) { v = swap16_add(v); return swap32_add(v); }
DI float wave_sum(float v) { v = dpp_add<0xB1>(v); v = dpp_add<0x4E>(v); v = dpp_add<0x124>(v); return sum_hi3(v); }

static constexpr u32 OFF_FLG = 256;
static constexpr u32 LN_FKV = 0, LN_FX = 8, LN_FST1 = 16, LN_FTT = 17, LN_FST2 = 18, LN_FACT = 20, LN_CNT = 40;
static constexpr u32 OFF_MODG = 32768;
static constexpr u32 OFF_MODP = OFF_MODG + 8192;
static constexpr u32 OFF_ST1 = OFF_MODP + 7 * 4 * 8 * 64 * 8;
static constexpr u32 OFF_ST2 = OFF_ST1 + 16384;
static constexpr u32 OFF_TT = OFF_ST2 + 16384;
static constexpr u32 OFF_TU = OFF_TT + 32768;
static constexpr u32 OFF_TX = OFF_TU + S * D * 2;
static constexpr u32 OFF_X1 = OFF_TX + S * D * 2;
static constexpr u32 OFF_Z = OFF_X1 + S * D * 2;
static constexpr u32 OFF_ACT = OFF_Z + S * D * 2;
static constexpr u32 WS_END = OFF_ACT + 64 * 112 * 1024;

static constexpr int KFROW = 4160, UROW = 286 * 16;
static constexpr int L_KF = 0;
static constexpr int L_P0 = 40960;
static constexpr int L_MP = L_P0 + 32768;
static constexpr int L_U = 114688;
static constexpr int L_RED = L_U + 2 * UROW, L_X = L_RED + 16384, L_S1 = 98304;
static constexpr int L_JUNK = 163840 - 2048;
static constexpr int L_MISC = 163840 - 1024;
static constexpr int L_E = 114688;
static constexpr int L_X1T = 147456, L_ST2 = L_X1T + 8192, L_TT = L_ST2 + 1024;
static constexpr int LDS_BYTES = 163840;
static_assert(L_X + 8192 <= L_JUNK && L_TT + 2048 <= L_JUNK, "lds");

#define SB() __builtin_amdgcn_sched_barrier(0)
#define BAR() do { asm volatile("s_waitcnt lgkmcnt(0)" ::: "memory"); __builtin_amdgcn_s_barrier(); asm volatile("" ::: "memory"); } while (0)
#define DRAIN() asm volatile("s_waitcnt vmcnt(0)" ::: "memory")
#define WAITV(n) asm volatile("s_waitcnt vmcnt(%0)" :: "i"(n) : "memory")
#define WAITL() asm volatile("s_waitcnt lgkmcnt(0)" ::: "memory")
#define BLD(dst, SRD, voff, soff, imm) asm volatile("buffer_load_dwordx4 %0, %1, %2, %3 offen offset:%4" : "=v"(dst) : "v"(voff), "s"(SRD), "s"(soff), "i"(imm))
#define BLD_SC1(dst, voff, soff, imm) asm volatile("buffer_load_dwordx4 %0, %1, %2, %3 offen offset:%4 sc1" : "=v"(dst) : "v"(voff), "s"(srd), "s"(soff), "i"(imm))
#define BLD2(dst, SRD, voff, soff) asm volatile("buffer_load_dwordx2 %0, %1, %2, %3 offen" : "=v"(dst) : "v"(voff), "s"(SRD), "s"(soff))
#define BLDU(dst, SRD, voff, imm) asm volatile("buffer_load_ushort %0, %1, %2, 0 offen offset:%3" : "=v"(dst) : "v"(voff), "s"(SRD), "i"(imm))
#define BLD1_SC1(dst, voff) asm volatile("buffer_load_dword %0, %1, %2, 0 offen sc1" : "=v"(dst) : "v"(voff), "s"(srd))
#define BLD2_SC1(dst, voff) asm volatile("buffer_load_dwordx2 %0, %1, %2, 0 offen sc1" : "=v"(dst) : "v"(voff), "s"(srd))
#define DMA(SRD, m0v, voff, soff) asm volatile("s_mov_b32 m0, %0\n\ts_nop 0\n\tbuffer_load_dwordx4 %1, %2, %3 offen lds" :: "s"(m0v), "v"(voff), "s"(SRD), "s"(soff) : "memory")
#define TOUCH(SRD, voff) asm volatile("s_mov_b32 m0, %0\n\ts_nop 0\n\tbuffer_load_dword %1, %2, 0 offen lds" :: "s"(lds0 + L_JUNK), "v"(voff), "s"(SRD) : "memory")
#define DMA_SC1(m0v, voff, soff) asm volatile("s_mov_b32 m0, %0\n\ts_nop 0\n\tbuffer_load_dwordx4 %1, %2, %3 offen sc1 lds" :: "s"(m0v), "v"(voff), "s"(srd), "s"(soff) : "memory")
template <int N> DI void waitv2(u32x4& a, u32x4& b) { asm volatile("s_waitcnt vmcnt(%2)" : "+v"(a), "+v"(b) : "i"(N)); }
DI u32x4 mksrd(const void* p, u32 bytes) { return (u32x4){(u32)(size_t)p, (u32)((size_t)p >> 32), bytes, 0x00020000u}; }
static constexpr u32 SPIN_CAP = 1u << 18;
#define CODE_BYTES 32768

DI void poll_eq(gu32* p, u32 want) {
#pragma unroll 1
    for (u32 spins = 0; spins < SPIN_CAP; ++spins) {
        const u32 f = __hip_atomic_load(p, RLX_AGENT);
        if (__all(f == want)) break;
        __builtin_amdgcn_s_sleep(2);
    }
    asm volatile("" ::: "memory");
}
DI float poll_granule(gu64* p, u32 tag) {
    u64 x = 0;
#pragma unroll 1
    for (u32 spins = 0; spins < SPIN_CAP; ++spins) {
        x = __hip_atomic_load(p, RLX_AGENT);
        if (__all((u32)(x >> 32) == tag)) break;
        __builtin_amdgcn_s_sleep(2);
    }
    return __uint_as_float((u32)x);
}
DI float sum_lo3(float v) { v = dpp_add<0xB1>(v); v = dpp_add<0x4E>(v); return dpp_add<0x141>(v); }
DI float silu_bf(float cv) { return rbf(cv * __builtin_amdgcn_rcpf(1.f + __builtin_amdgcn_exp2f(cv * -1.44269504f))); }
DI f32x2 rbf2(f32x2 a) { const u32 p = pk2(a.x, a.y); return (f32x2){lo2f(p), hi2f(p)}; }
DI f32x2 up2(u32 p) { return (f32x2){lo2f(p), hi2f(p)}; }

DI u32 row_stats(u32x4 xr, int lane) {
    float e[8];
#pragma unroll
    for (int jx = 0; jx < 4; ++jx) { e[2 * jx] = lo2f(xr[jx]); e[2 * jx + 1] = hi2f(xr[jx]); }
    float s = ((e[0] + e[1]) + (e[2] + e[3])) + ((e[4] + e[5]) + (e[6] + e[7]));
    if (lane >= 56) s = 0.f;
    const float m = sum_hi3(sum_lo3(s)) * (1.f / D);
    float qv = 0.f;
#pragma unroll
    for (int jx = 0; jx < 8; ++jx) qv += (e[jx] - m) * (e[jx] - m);
    if (lane >= 56) qv = 0.f;
    return pk2(m, __builtin_sqrtf(rbf(rbf(sum_hi3(sum_lo3(qv)) * (1.f / D)) + 1e-5f)));
}
template <int V> struct IC { static constexpr int value = V; };
constexpr int conv_tile(int tg, int i) { return tg ? (i == 0 ? 1 : i == 1 ? 2 : i == 2 ? 5 : 6) : (i == 0 ? 0 : i == 1 ? 3 : i == 2 ? 4 : 7); }
constexpr int conv_cnt(int tg, int x, int i) {
    const int np = tg ? 7 : 8; int c = 0;
    for (int xx = 0; xx <= x; ++xx) for (int ii = 0; ii < 4; ++ii) { if (xx == x && ii >= i) break; if (xx % np <= conv_tile(tg, ii)) ++c; }
    return c;
}
static constexpr int CPD = 6;
#define WPRE 4
#define G1MAP 0
#define AUXDELAY 40
#define TPACE 0
#define R1N 0
template <int TG, int X, int I, typename FN>
DI void conv_mm(const u32x4 (&A)[4][8], u32x4 (&B)[CPD + 1], f32x4 (&acc)[4], FN& hook) {
    constexpr int NP = TG ? 7 : 8, T = conv_tile(TG, I), cnt = conv_cnt(TG, X, I);
    if constexpr (X % NP <= T) {
        acc[I] = mfma16(A[X / NP][T - X % NP], B[X % (CPD + 1)], acc[I]);
        if constexpr (cnt & 1) { SB(); hook(IC<(cnt >> 1)>{}); SB(); }
    }
}
template <int TG, int X, typename FN>
DI void conv_step(const u32x4 (&A)[4][8], const char* bbase, u32x4 (&B)[CPD + 1], f32x4 (&acc)[4], FN& hook) {
    constexpr int NP = TG ? 7 : 8, Y = X + CPD;
    if constexpr (Y < 4 * NP) B[Y % (CPD + 1)] = *(const u32x4*)(bbase + 192 - 64 * (Y / NP) + 512 * (Y % NP));
    if constexpr (X >= 0) { conv_mm<TG, X, 0>(A, B, acc, hook); conv_mm<TG, X, 1>(A, B, acc, hook); conv_mm<TG, X, 2>(A, B, acc, hook); conv_mm<TG, X, 3>(A, B, acc, hook); }
    if constexpr (X + 1 < 4 * NP) conv_step<TG, X + 1>(A, bbase, B, acc, hook);
}
template <int TG, typename FN>
DI void conv_main(const u32x4 (&A)[4][8], const char* bbase, f32x4 (&acc)[4], FN& hook) {
    u32x4 B[CPD + 1];
    conv_step<TG, -CPD>(A, bbase, B, acc, hook);
    SB();
}

__global__ __launch_bounds__(512, 2) void fused(const u16* __restrict__ q, const u16* __restrict__ k, const u16* __restrict__ v,
                                                const u16* __restrict__ shk, const u16* __restrict__ ssmk, const u16* __restrict__ ssmD,
                                                const u16* __restrict__ wg, const u16* __restrict__ wu, const u16* __restrict__ wd,
                                                const u16* __restrict__ c, const u16* __restrict__ wmod, char* __restrict__ ws, u16* __restrict__ out) {
    extern __shared__ __attribute__((aligned(16))) char smem[];
    const int b = blockIdx.x, tid = threadIdx.x, lane = tid & 63;
    const int wave = __builtin_amdgcn_readfirstlane(tid >> 6);
    ST_DECL ST(0);
    const char* pc0;
    asm volatile("s_getpc_b64 %0" : "=s"(pc0));
    asm volatile("" :: "s"(q), "s"(k), "s"(v), "s"(shk), "s"(ssmk), "s"(ssmD), "s"(wg), "s"(wu), "s"(wd), "s"(c), "s"(wmod), "s"(ws), "s"(out));
    u32 epoch;
#define LOAD_EPOCH() epoch = __hip_atomic_load((gu32*)ws, RLX_AGENT)
#define GET_TAG() const u32 tag = __builtin_amdgcn_readfirstlane(epoch) * 0x9E3779B1u + 0x7F4A7C15u
    gu32* flg = (gu32*)(ws + OFF_FLG);
    gu64* modg = (gu64*)(ws + OFF_MODG);
    gu64* modp = (gu64*)(ws + OFF_MODP);
    const __amdgpu_buffer_rsrc_t rsrc = __builtin_amdgcn_make_buffer_rsrc(ws, 0, WS_END, 0x00020000);
    const u32x4 srd = mksrd(ws, WS_END);
    const int n = lane & 15, g = lane >> 4;

    if (b >= 224) {
        const int a = b - 224;
        float* red = (float*)(smem + 8192);
        if (a == 0 && wave == 0) __hip_atomic_store(flg + (LN_CNT + 2 * lane) * 32, 0u, RLX_AGENT);
        const u32x4 xr = *(const u32x4*)(q + (size_t)(8 * b + wave) * D + (lane < 56 ? lane : 55) * 8);
        LOAD_EPOCH();
        DRAIN();
        GET_TAG();
        {
            const u32 rs = row_stats(xr, lane);
            if (lane == 0) __hip_atomic_store((gu64*)(ws + OFF_ST1) + 8 * b + wave, ((u64)tag << 32) | rs, RLX_AGENT);
        }
        __builtin_amdgcn_s_sleep(AUXDELAY);
        ST(1);
        if (a >= 18) {
            const int blk = a - 18, cb = blk < 7 ? 21 + blk : 28 + blk;
            u32x4 w[7]; u16 cr[7];
#pragma unroll
            for (int i = 0; i < 7; ++i) { cr[i] = c[(tid >> 3) + 64 * i]; w[i] = *(const u32x4*)(wmod + (size_t)((tid >> 3) + 64 * i) * (6 * D) + cb * 64 + (tid & 7) * 8); }
            float acc[8] = {0, 0, 0, 0, 0, 0, 0, 0};
#pragma unroll
            for (int i = 0; i < 7; ++i) {
                const float sc = silu_bf(bf2f(cr[i]));
#pragma unroll
                for (int jx = 0; jx < 4; ++jx) { acc[2 * jx] += sc * lo2f(w[i][jx]); acc[2 * jx + 1] += sc * hi2f(w[i][jx]); }
            }
#pragma unroll
            for (int jx = 0; jx < 8; ++jx) acc[jx] = sum_hi3(acc[jx]);
            if (lane < 8) { *(f32x4*)(red + wave * 64 + lane * 8) = (f32x4){acc[0], acc[1], acc[2], acc[3]}; *(f32x4*)(red + wave * 64 + lane * 8 + 4) = (f32x4){acc[4], acc[5], acc[6], acc[7]}; }
            BAR();
            if (tid < 64) {
                float s = 0.f;
#pragma unroll
                for (int i = 0; i < 8; ++i) s += red[i * 64 + tid];
                __hip_atomic_store(modg + blk * 64 + tid, ((u64)tag << 32) | __float_as_uint(rbf(s)), RLX_AGENT);
            }
            BAR();
        }
        ST(2);
        if (a < 28) {
            const int type = a / 14, sl = a % 14, rg = tid >> 4, c16 = tid & 15;
            const u16* W = (type ? wu : wg) + sl * 128 + c16 * 8;
            u32x4 w[14];
#pragma unroll
            for (int i = 0; i < 14; ++i) w[i] = *(const u32x4*)(W + (size_t)(rg + 32 * i) * F);
            if (wave < 7) {
                u64 pp[8];
#pragma unroll 1
                for (u32 spins = 0; spins < SPIN_CAP; ++spins) {
                    bool ok = true;
#pragma unroll
                    for (int p = 0; p < 8; ++p) { pp[p] = __hip_atomic_load(modp + ((wave * 4 + 3) * 8 + p) * 64 + lane, RLX_AGENT); ok &= (u32)(pp[p] >> 32) == tag; }
                    if (__all(ok)) break;
                    __builtin_amdgcn_s_sleep(4);
                }
#define PV(i) __uint_as_float((u32)pp[i])
                const float cbv = ((PV(0) + PV(1)) + (PV(2) + PV(3))) + ((PV(4) + PV(5)) + (PV(6) + PV(7)));
                const float sbv = poll_granule(modg + tid, tag);
                *(f32x2*)(smem + 2048 + tid * 8) = (f32x2){rbf(1.f + rbf(cbv)), sbv};
            }
            BAR();
            ST(3);
            float a1[8] = {0, 0, 0, 0, 0, 0, 0, 0}, a2[8] = {0, 0, 0, 0, 0, 0, 0, 0};
#pragma unroll
            for (int i = 0; i < 14; ++i) {
                const f32x2 s = *(const f32x2*)(smem + 2048 + (rg + 32 * i) * 8);
#pragma unroll
                for (int jx = 0; jx < 4; ++jx) {
                    const float l = lo2f(w[i][jx]), h = hi2f(w[i][jx]);
                    a1[2 * jx] += l * s.x; a1[2 * jx + 1] += h * s.x; a2[2 * jx] += l * s.y; a2[2 * jx + 1] += h * s.y;
                }
            }
#pragma unroll
            for (int jx = 0; jx < 8; ++jx) { a1[jx] = sum_hi2(a1[jx]); a2[jx] = sum_hi2(a2[jx]); }
            if (lane < 16)
#pragma unroll
                for (int jx = 0; jx < 4; ++jx) *(f32x4*)(red + wave * 256 + lane * 16 + jx * 4) = (f32x4){a1[2 * jx], a2[2 * jx], a1[2 * jx + 1], a2[2 * jx + 1]};
            BAR();
            if (tid < 128) {
                f32x2 s = {0.f, 0.f};
#pragma unroll
                for (int i = 0; i < 8; ++i) s += *(const f32x2*)(red + i * 256 + tid * 2);
                __hip_atomic_store((gu64*)(ws + OFF_TT) + type * F + sl * 128 + tid, __builtin_bit_cast(u64, s), RLX_AGENT);
            }
            DRAIN();
            BAR();
            if (tid == 0) __hip_atomic_store(flg + LN_FTT * 32 + a, tag, RLX_AGENT);
        }
        ST(4);
        if (wave == 0) { poll_granule((gu64*)(ws + OFF_ST1) + 1792 + 8 * (lane & 31), tag); poll_eq(flg + (LN_CNT + 2 * ((b & 7) * 8 + (lane & 7))) * 32, 28u); }
        BAR();
        ST(5);
        {
            const int ro = tid & 7, ch = tid >> 3;
            u32x4 w[7];
#pragma unroll
            for (int i = 0; i < 7; ++i) w[i] = __builtin_amdgcn_raw_buffer_load_b128(rsrc, OFF_X1 + ((a >> 1) * 448 + ch + 64 * i) * 256 + (a & 1) * 128 + ro * 16, 0, 16);
            float a1[8] = {0, 0, 0, 0, 0, 0, 0, 0}, a2[8] = {0, 0, 0, 0, 0, 0, 0, 0};
#pragma unroll
            for (int i = 0; i < 7; ++i)
#pragma unroll
                for (int jx = 0; jx < 4; ++jx) {
                    const float l = lo2f(w[i][jx]), h = hi2f(w[i][jx]);
                    a1[2 * jx] += l; a1[2 * jx + 1] += h; a2[2 * jx] += l * l; a2[2 * jx + 1] += h * h;
                }
#pragma unroll
            for (int jx = 0; jx < 8; ++jx) { a1[jx] = sum_hi3(a1[jx]); a2[jx] = sum_hi3(a2[jx]); }
            if (lane < 8)
#pragma unroll
                for (int jx = 0; jx < 4; ++jx) *(f32x4*)(red + wave * 128 + lane * 16 + jx * 4) = (f32x4){a1[2 * jx], a2[2 * jx], a1[2 * jx + 1], a2[2 * jx + 1]};
            BAR();
            if (wave == 0) {
                f32x2 s = {0.f, 0.f};
#pragma unroll
                for (int i = 0; i < 8; ++i) s += *(const f32x2*)(red + i * 128 + lane * 2);
                const float m = s.x * (1.f / D);
                const float rs = 1.f / rbf(__builtin_sqrtf(rbf(rbf(fmaxf(s.y * (1.f / D) - m * m, 0.f)) + 1e-5f)));
                __hip_atomic_store((gu64*)(ws + OFF_ST2) + 64 * a + lane, __builtin_bit_cast(u64, (f32x2){rs, -rbf(m) * rs}), RLX_AGENT);
                DRAIN();
                if (lane == 0) {
                    __hip_atomic_store(flg + LN_FST2 * 32 + a, tag, RLX_AGENT);
                    if (a == 0) __hip_atomic_store((gu32*)ws, tag, RLX_AGENT);
                }
            }
        }
        ST(6);
        ST_FLUSH();
        return;
    }

    const int gq = b >> 5, j = b & 31;
    const int ip = (j & 7) * 4 + (j >> 3), d0 = 64 * gq + 2 * ip;
    const int xc = b & 7, ii = b >> 3;
    const int rt = 4 * (xc >> 1) + (ii & 3), sl = 7 * (xc & 1) + (ii >> 2);
    const int rb = 8 * (xc >> 1) + 4 * (xc & 1) + (ii & 3), db = ii >> 2;
    const int ch = wave >> 2, rh = (wave >> 1) & 1, tg = wave & 1;
    const u32 lds0 = (u32)(size_t)(LAS char*)smem;
    const u32x4 srdg = mksrd(wg, D * F * 2), srdu = mksrd(wu, D * F * 2), srdd = mksrd(wd, D * F * 2);
    const u32 tl_row = lane >> 3, tl_st = (((lane >> 1) & 3) ^ (lane >> 4)) * 32 + (lane & 1) * 16;
    const u32 w1voff = tl_row * (F * 2) + tl_st;
    const u32 l16 = lane * 16;
#define TRB(st) (smem + (g >> 1) * 1024 + ((g & 1) * 4 + (n >> 2)) * 128 + (((st) ^ ((g & 1) * 2 + (n >> 3))) * 32) + (lane & 3) * 8)
    const int t0 = tg ? (rh ? 5 : 1) : (rh ? 4 : 0), t1 = tg ? (rh ? 6 : 2) : (rh ? 7 : 3);
    volatile LAS u32* lcnt = (volatile LAS u32*)(smem + L_MISC);

    const u32x4 srdk = mksrd(k, S * D * 2), srdv = mksrd(v, S * D * 2), srdq = mksrd(q, S * D * 2), srdf = mksrd(ssmk, S * D * 2),
                srdm = mksrd(wmod, D * 6 * D * 2), srdc = mksrd(c, D * 2), srds = mksrd(shk, D * 8);
    const u32 p0l = lds0 + L_P0 + wave * 4096;
    const char* p0r = smem + L_P0 + wave * 4096 + (n >> 2) * 128 + g * 32 + (lane & 3) * 8;
    const u32 tvo = (64 * j + 8 * wave + (lane >> 3)) * (D * 2) + gq * 128 + (lane & 7) * 16;
    u32x4 kw, mw, xrow; u32x2 pv, sw; u32 cr;
    const int kc = tid & 255;
    {
        DMA(srdk, p0l + 1024, tvo, 0);
        DMA(srdv, p0l + 2048, tvo, 0);
        DMA(srdk, p0l, tvo - 8 * D * 2, 0);
        BLD2(sw, srds, (u32)(lane * 8), gq * 512);
        BLD1_SC1(epoch, 0u * lane);
        if (wave == 4) { const u32x4 srdp = mksrd(pc0, 30720); TOUCH(srdp, (u32)(((b >> 3) * 64 + lane) * 16)); }
        if (tid < 60) *(u32x4*)(smem + L_U + (tid / 30) * UROW + (tid % 30) * 16) = (u32x4){0, 0, 0, 0};
        if (tid >= 64 && tid < 80) *(u32x4*)(smem + L_KF + ((tid - 64) >> 1) * KFROW + (tid & 1) * 16) = (u32x4){0, 0, 0, 0};
        if (tid == 128) { lcnt[1] = 0; lcnt[2] = 0; }
        asm volatile("s_waitcnt vmcnt(0)" : "+v"(sw), "+v"(epoch) :: "memory");
        ST(1);
        const u32 ko = tid * 16, po = kc ? ko - 8 : ko;
        BLD(kw, srdf, ko, d0 * 4096, 0); BLD2(pv, srdf, po, d0 * 4096);
        const u32x2 ka = trread(p0r + 512), kb2 = trread(p0r + 1024), kc2 = trread(p0r + 1536), va = trread(p0r + 2048), vb = trread(p0r + 2560);
        const float kk[11] = {hi2f(ka.x), lo2f(ka.y), hi2f(ka.y), lo2f(kb2.x), hi2f(kb2.x), lo2f(kb2.y), hi2f(kb2.y), lo2f(kc2.x), hi2f(kc2.x), lo2f(kc2.y), hi2f(kc2.y)};
        const u32 vv[4] = {va.x, va.y, vb.x, vb.y};
        const float w0 = lo2f(sw.x), w1 = hi2f(sw.x), w2 = lo2f(sw.y), w3 = hi2f(sw.y);
        u32x4 o;
#pragma unroll
        for (int e = 0; e < 4; ++e) {
            f32x2 s = rbf2((f32x2){kk[2 * e], kk[2 * e + 1]} * w3);
            s = rbf2(s + rbf2((f32x2){kk[2 * e + 1], kk[2 * e + 2]} * w2));
            s = rbf2(s + rbf2((f32x2){kk[2 * e + 2], kk[2 * e + 3]} * w1));
            s = rbf2(s + rbf2((f32x2){kk[2 * e + 3], kk[2 * e + 4]} * w0));
            s *= up2(vv[e]);
            o[3 - e] = pk2(s.y, s.x);
        }
        __builtin_amdgcn_raw_buffer_store_b128(o, rsrc, OFF_TU + (gq * 256 + j * 8 + wave) * 1024 + l16, 0, 16);
        SB();
        WAITV(1);
        asm volatile("" : "+v"(kw), "+v"(pv));
        if (kc == 0) pv = (u32x2){0, 0};
        char* kd = smem + L_KF + ch * 4 * KFROW + 32 + kc * 16;
        const u32 s0 = __builtin_amdgcn_alignbit(pv.y, pv.x, 16), s1 = __builtin_amdgcn_alignbit(kw.x, pv.y, 16), s2 = __builtin_amdgcn_alignbit(kw.y, kw.x, 16),
                  s3 = __builtin_amdgcn_alignbit(kw.z, kw.y, 16), s4 = __builtin_amdgcn_alignbit(kw.w, kw.z, 16);
        *(u32x4*)(kd) = kw;
        *(u32x4*)(kd + KFROW) = (u32x4){s1, s2, s3, s4};
        *(u32x4*)(kd + 2 * KFROW) = (u32x4){pv.y, kw.x, kw.y, kw.z};
        *(u32x4*)(kd + 3 * KFROW) = (u32x4){s0, s1, s2, s3};
        DRAIN();
    }
    GET_TAG();
    BAR();
    if (tid == 0) __hip_atomic_store(flg + (LN_FKV + gq) * 32 + j, tag, RLX_AGENT);
    ST(2);
    u32x4 A[4][8];
    {
        DMA(srdq, p0l + 3072, tvo, 0);
        const int qty = j >> 3, part = j & 7, cb = (qty == 3 ? 28 : 7 * qty) + gq, mr = 56 * part + 8 * (wave < 7 ? wave : 6) + (lane >> 3);
        BLD(mw, srdm, (u32)(mr * (6 * D * 2) + (lane & 7) * 16), cb * 128, 0);
        BLDU(cr, srdc, (u32)(mr * 2), 0);
        BLD(xrow, srdq, l16, (8 * b + wave) * (D * 2), 0);
        const char* abase = smem + L_KF + (ch * 4 + ((3 - n) & 3)) * KFROW + 56 + 8 * (n >> 2) - 16 * g + 64 * 4 * rh;
#pragma unroll
        for (int r = 0; r < 4; ++r)
#pragma unroll
            for (int p = 0; p < 8; ++p) {
                const u32x2 l = *(const volatile LAS u32x2*)(abase + 64 * r + 512 * p), h = *(const volatile LAS u32x2*)(abase + 64 * r + 512 * p + 8);
                A[r][p] = (u32x4){l.x, l.y, h.x, h.y};
            }
        SB();
        asm volatile("s_waitcnt vmcnt(0)" : "+v"(mw), "+v"(cr), "+v"(xrow) :: "memory");
        const u32x2 xa = trread(p0r + 3072), xb = trread(p0r + 3584);
        __builtin_amdgcn_raw_buffer_store_b128((u32x4){xa.x, xa.y, xb.x, xb.y}, rsrc, OFF_TX + (gq * 256 + j * 8 + wave) * 1024 + l16, 0, 16);
        const u32 rs1 = row_stats(xrow, lane);
        if (lane == 0) __hip_atomic_store((gu64*)(ws + OFF_ST1) + 8 * b + wave, ((u64)tag << 32) | rs1, RLX_AGENT);
        const float sc = silu_bf(lo2f(cr));
        float mine = 0.f;
#pragma unroll
        for (int jx = 0; jx < 8; ++jx) { const float s = sum_hi3(sc * ((jx & 1) ? hi2f(mw[jx >> 1]) : lo2f(mw[jx >> 1]))); if ((lane >> 3) == jx) mine = s; }
        *(float*)(smem + L_MP + wave * 256 + ((lane & 7) * 8 + (lane >> 3)) * 4) = mine;
    }
    BAR();
    f32x4 y0, y1;
    const int d = d0 + ch;
    {
        if (wave == 7) {
            const float* mp = (const float*)(smem + L_MP) + lane;
            const float s = (((mp[0] + mp[64]) + (mp[128] + mp[192])) + (mp[256] + mp[320])) + mp[384];
            __hip_atomic_store(modp + (gq * 32 + j) * 64 + lane, ((u64)tag << 32) | __float_as_uint(s), RLX_AGENT);
        }
        ST(3);
        u32 fl; u32x2 mg;
        const u32 flo = OFF_FLG + (LN_FX + gq) * 128 + (lane & 31) * 4;
        const u32 mgo = OFF_MODP + ((gq * 32 + g * 8 + (lane & 7)) * 64 + 2 * ip + ((lane >> 3) & 1)) * 8;
        DRAIN();
        if (lane == 0 && __hip_atomic_fetch_add((LAS u32*)lcnt + 1, 1u, __ATOMIC_RELAXED, __HIP_MEMORY_SCOPE_WORKGROUP) == 7u)
            __hip_atomic_store(flg + (LN_FX + gq) * 32 + j, tag, RLX_AGENT);
        if (wave >= 4) {
            DMA(srdg, lds0 + L_JUNK, l16, b * 7168 + (wave & 3) * 1024);
            DMA(srdg, lds0 + L_JUNK, l16, b * 7168 + 3072 + (wave & 3) * 1024);
            DMA(srdu, lds0 + L_JUNK, l16, b * 7168 + (wave & 3) * 1024);
            DMA(srdu, lds0 + L_JUNK, l16, b * 7168 + 3072 + (wave & 3) * 1024);
            ST(4);
        } else {
            if (wave == 0) { poll_eq(flg + (LN_FKV + gq) * 32 + (lane & 31), tag); lcnt[2] = 1; }
            else {
#pragma unroll 1
                for (u32 spins = 0; spins < (SPIN_CAP << 4) && lcnt[2] == 0; ++spins) __builtin_amdgcn_s_sleep(1);
            }
            asm volatile("" ::: "memory");
            ST(4);
            u32x4 kv0, kv1;
            const u32 vo = (tid >> 1) * 1024 + (tid & 1) * 16;
            BLD_SC1(kv0, vo, OFF_TU + gq * 262144 + ip * 32, 0);
            BLD_SC1(kv1, vo, OFF_TU + gq * 262144 + 131072 + ip * 32, 0);
            waitv2<0>(kv0, kv1);
            char* dst = smem + L_U + (tid & 1) * UROW + (30 + (tid >> 1)) * 16;
            *(u32x4*)dst = kv0; *(u32x4*)(dst + 2048) = kv1;
            BLD1_SC1(fl, flo);
            BLD2_SC1(mg, mgo);
        }
        BAR();
        ST(5);
        f32x4 acc[4] = {{0, 0, 0, 0}, {0, 0, 0, 0}, {0, 0, 0, 0}, {0, 0, 0, 0}};
        const char* bbase = smem + L_U + ch * UROW + (28 + 2 * n + g) * 16 - 64 * 4 * rh - 192;
        auto hook = [&](auto ic) __attribute__((always_inline)) {
            constexpr int i = decltype(ic)::value;
            if constexpr (i == 16) if (wave < 4) {
                u32 f1; u32x2 m2;
                asm volatile("s_waitcnt vmcnt(0)\n\tv_mov_b32 %0, %2\n\tv_mov_b64 %1, %3" : "=&v"(f1), "=&v"(m2) : "v"(fl), "v"(mg) : "memory");
                if (!__all(f1 == tag)) poll_eq((gu32*)(ws + flo), tag);
                const u32 xo = (lane >> 1) * 1024 + (lane & 1) * 16;
#pragma unroll
                for (int e = 0; e < 2; ++e) {
                    DMA_SC1(lds0 + L_X + (wave * 2 + e) * 1024, xo, OFF_TX + gq * 262144 + (wave * 2 + e) * 32768 + ip * 32);
                    DMA_SC1(lds0 + L_S1 + (wave * 4 + e * 2) * 1024, l16, OFF_ST1 + (wave * 4 + e * 2) * 1024);
                    DMA_SC1(lds0 + L_S1 + (wave * 4 + e * 2 + 1) * 1024, l16, OFF_ST1 + (wave * 4 + e * 2 + 1) * 1024);
                }
                if (wave == 0) {
                    float mv = __uint_as_float(m2.x);
                    if (!__all(m2.y == tag)) mv = poll_granule((gu64*)(ws + mgo), tag);
                    mv = sum_lo3(mv);
                    if ((lane & 7) == 0) *(float*)(smem + L_MISC + 16 + ((lane >> 3) & 1) * 16 + g * 4) = rbf(mv);
                }
            }
        };
        if (tg) conv_main<1>(A, bbase, acc, hook); else conv_main<0>(A, bbase, acc, hook);
        ST(6);
        f32x4* red = (f32x4*)(smem + L_RED);
        red[(wave * 2 + 0) * 64 + lane] = rh ? acc[0] : acc[2];
        red[(wave * 2 + 1) * 64 + lane] = rh ? acc[1] : acc[3];
        y0 = rh ? acc[2] : acc[0]; y1 = rh ? acc[3] : acc[1];
    }
    u32x4 W[14][2];
    const char* wrb = TRB(wave & 3) + (wave >> 2) * 4096;
    {
        const float Dv = bf2f(ssmD[d]);
        DRAIN();
        BAR();
        ST(7);
        const f32x4 mm = *(const f32x4*)(smem + L_MISC + 16 + ch * 16);
        const float sa = mm.x, ca1 = rbf(1.f + mm.y), ga = mm.z, cb1 = rbf(1.f + mm.w);
        const f32x4* red = (const f32x4*)(smem + L_RED);
        const int pw = wave ^ 2;
        y0 += red[(pw * 2 + 0) * 64 + lane];
        y1 += red[(pw * 2 + 1) * 64 + lane];
        u32 x1p[2][2], zp[2][2];
#pragma unroll
        for (int i = 0; i < 2; ++i) {
            const f32x4 y = i ? y1 : y0;
            const int s0 = 256 * (i ? t1 : t0) + 16 * n + 4 * g;
            const u32x2 ur = *(const u32x2*)(smem + L_U + ch * UROW + (30 + (s0 >> 3)) * 16 + ((s0 & 4) ? 0 : 8));
            const u32x2 xr = *(const u32x2*)(smem + L_X + (s0 >> 3) * 32 + ch * 16 + (s0 & 4) * 2);
            u32x4 g0 = *(const u32x4*)(smem + L_S1 + s0 * 8), g1 = *(const u32x4*)(smem + L_S1 + s0 * 8 + 16);
            if (!__all(g0.y == tag && g0.w == tag && g1.y == tag && g1.w == tag)) {
                gu64* gp = (gu64*)(ws + OFF_ST1) + s0;
                g0.x = __float_as_uint(poll_granule(gp, tag)); g0.z = __float_as_uint(poll_granule(gp + 1, tag));
                g1.x = __float_as_uint(poll_granule(gp + 2, tag)); g1.z = __float_as_uint(poll_granule(gp + 3, tag));
            }
            const u32x4 st = {g0.x, g0.z, g1.x, g1.z};
#pragma unroll
            for (int e = 0; e < 2; ++e) {
                const f32x2 x = up2(xr[e]), u = {hi2f(ur[1 - e]), lo2f(ur[1 - e])};
                const f32x2 m = {lo2f(st[2 * e]), lo2f(st[2 * e + 1])}, rd = {__builtin_amdgcn_rcpf(hi2f(st[2 * e])), __builtin_amdgcn_rcpf(hi2f(st[2 * e + 1]))};
                const f32x2 ln = rbf2(rbf2(x - m) * rd);
                const f32x2 h = rbf2(rbf2(ln * ca1) + sa);
                const f32x2 yy = rbf2(rbf2((f32x2){y[2 * e], y[2 * e + 1]}) + rbf2(u * Dv));
                const f32x2 o = x + rbf2(rbf2(h * yy) * ga);
                x1p[i][e] = pk2(o.x, o.y);
                const f32x2 z = up2(x1p[i][e]) * cb1;
                zp[i][e] = pk2(z.x, z.y);
            }
        }
        {
            const auto z0 = __builtin_amdgcn_permlane16_swap(zp[0][0], zp[1][0], false, false), z1 = __builtin_amdgcn_permlane16_swap(zp[0][1], zp[1][1], false, false);
            const auto a0 = __builtin_amdgcn_permlane16_swap(x1p[0][0], x1p[1][0], false, false), a1 = __builtin_amdgcn_permlane16_swap(x1p[0][1], x1p[1][1], false, false);
            const int s0 = 256 * ((g & 1) ? t1 : t0) + 16 * n + 8 * (g >> 1);
            const u32 off = ((s0 >> 7) * 448 + d) * 256 + (s0 & 127) * 2;
            __builtin_amdgcn_raw_buffer_store_b128((u32x4){z0[0], z1[0], z0[1], z1[1]}, rsrc, OFF_Z + off, 0, 16);
            __builtin_amdgcn_raw_buffer_store_b128((u32x4){a0[0], a1[0], a0[1], a1[1]}, rsrc, OFF_X1 + off, 0, 16);
        }
        SB();
    }
    const u32x4 srdw = wave < 4 ? srdg : srdu;
#define W1DMA(ks, slot) do { _Pragma("unroll") for (int hf = 0; hf < 2; ++hf) { \
        const u32 m0v = lds0 + ((slot) * 16 + (wave >> 2) * 8 + hf * 4 + (wave & 3)) * 1024; \
        const u32 so = (32 * (ks) + 8 * (wave & 3)) * (F * 2) + sl * 256 + hf * 128; \
        DMA(srdw, m0v, w1voff, so); } } while (0)
#define W1RD(ks, slot) do { W[ks][0] = trfrag(wrb + (slot) * 16384); W[ks][1] = trfrag(wrb + (slot) * 16384 + 8192); } while (0)
#pragma unroll
    for (int ks = 0; ks < WPRE; ++ks) W1DMA(ks, ks);
    WAITV(2 * WPRE);
    BAR();
    if (wave == 0 && lane < 8) __hip_atomic_fetch_add(flg + (LN_CNT + 2 * (lane * 8 + (b & 7))) * 32, 1u, RLX_AGENT);
    ST(8);
#pragma unroll
    for (int ks = WPRE; ks < 9; ++ks) W1DMA(ks, ks);
    DRAIN();
    BAR();
#pragma unroll
    for (int ks = 0; ks < 9; ++ks) W1RD(ks, ks);
    ST(9);
    if (wave == 0) poll_eq(flg + (LN_CNT + 2 * (((b >> 3) & 7) * 8 + (lane & 7))) * 32, 28u);
    BAR();
    ST(10);
    const u32 zvoff = tl_row * 256 + tl_st;
    const u32 w2voff = tl_row * (D * 2) + tl_st;
    const u32 w2so = (224 * wave) * (D * 2) + db * 128;
#define W2DMA(ks, ldsoff) do { _Pragma("unroll") for (int i_ = 0; i_ < 4; ++i_) DMA(srdd, lds0 + (ldsoff) + i_ * 1024, w2voff, w2so + ((ks) * 4 + i_) * 8 * (D * 2)); } while (0)
#define W2RD(ks, ldsoff) do { _Pragma("unroll") for (int t_ = 0; t_ < 4; ++t_) { const char* p_ = smem + (ldsoff) + g * 1024 + (n >> 2) * 128 + (lane & 3) * 8; \
        const u32x2 l_ = trread(p_ + ((t_ ^ (n >> 3)) * 32)), h_ = trread(p_ + 512 + ((t_ ^ (2 + (n >> 3))) * 32)); W2[ks][t_] = (u32x4){l_.x, l_.y, h_.x, h_.y}; } } while (0)
#define ZDMA(i) DMA_SC1(lds0 + (((i) / 7) * 56 + ((i) % 7) * 8 + wave) * 1024, zvoff, OFF_Z + (rt * 448 + (((i) % 7) * 8 + wave) * 8) * 256 + ((i) / 7) * 128)
#pragma unroll
    for (int i = 0; i < 7; ++i) ZDMA(i);
    f32x4 acc[8][2];
#pragma unroll
    for (int s = 0; s < 8; ++s) { acc[s][0] = (f32x4){0, 0, 0, 0}; acc[s][1] = (f32x4){0, 0, 0, 0}; }
    const int ew2 = L_E + wave * 4096, ha2 = wave * 7168, hb2 = 57344 + wave * 7168;
    {
        u32x4 B[2][4];
        u32 fl2;
        const u32 flo2 = OFF_FLG + ((lane & 2) ? LN_FST2 * 128 + (2 * rt + (lane & 1)) * 4 : LN_FTT * 128 + ((lane & 1) * 14 + sl) * 4);
#pragma unroll
        for (int jj = -1; jj < 28; ++jj) {
            const int nx = jj + 1;
            if (nx == 0) { WAITV(6); BAR(); }
            if (nx == 2) { WAITV(8); BAR(); }
            if (nx == 6) { WAITV(10); BAR(); }
            if (nx == 8) {
                WAITV(1); BAR();
#pragma unroll
                for (int ks = 9; ks < 14; ++ks) { W[ks][0] = trfrag(wrb + 57344 + (ks - 9) * 16384); W[ks][1] = trfrag(wrb + 57344 + (ks - 9) * 16384 + 8192); }
            }
            if (nx == 9) BAR();
            if (nx == 14) { WAITV(3); BAR(); }
            if (nx == 18) { WAITV(1); BAR(); }
            if (nx >= 0 && nx < 5) {
#pragma unroll
                for (int hf = 0; hf < 2; ++hf) DMA(srdw, lds0 + 57344 + (nx * 16 + (wave >> 2) * 8 + hf * 4 + (wave & 3)) * 1024, w1voff, (32 * (nx + 9) + 8 * (wave & 3)) * (F * 2) + sl * 256 + hf * 128);
            }
            if (nx == 6) DMA(srdd, lds0 + L_JUNK, l16, b * 7168 + wave * 1024);
            if (nx >= 9 && nx < 12) { ZDMA(2 * nx - 11); ZDMA(2 * nx - 10); }
            if (nx == 12) ZDMA(13);
            if (nx == 15) DMA_SC1(lds0 + L_X1T + wave * 1024, zvoff, OFF_X1 + ((rb >> 1) * 448 + db * 64 + wave * 8) * 256 + (rb & 1) * 128);
            if (nx == 18 && wave == 0) BLD1_SC1(fl2, flo2);
            if (nx == 21 && wave == 0) {
                asm volatile("s_waitcnt vmcnt(0)" : "+v"(fl2) :: "memory");
                if (!__all(fl2 == tag)) poll_eq((gu32*)(ws + flo2), tag);
                DMA_SC1(lds0 + L_ST2, l16, OFF_ST2 + rt * 1024);
                DMA_SC1(lds0 + L_TT, l16, OFF_TT + sl * 1024);
                DMA_SC1(lds0 + L_TT + 1024, l16, OFF_TT + F * 8 + sl * 1024);
            }
            if (nx >= 21 && nx < 25) { DMA(srdd, lds0 + ew2 + (nx - 21) * 1024, w2voff, w2so + (nx - 21) * 8 * (D * 2)); DMA(srdd, lds0 + ha2 + (nx - 21) * 1024, w2voff, w2so + (nx - 17) * 8 * (D * 2)); }
            if (nx < 28)
#pragma unroll
                for (int t = 0; t < 4; ++t) B[nx & 1][t] = trfrag(TRB(t) + (nx / 14) * 57344 + (nx % 14) * 4096);
            if (jj >= 0) {
#pragma unroll
                for (int t = 0; t < 4; ++t) {
                    const int s = (jj / 14) * 4 + t;
                    acc[s][0] = mfma16(W[jj % 14][0], B[jj & 1][t], acc[s][0]);
                    acc[s][1] = mfma16(W[jj % 14][1], B[jj & 1][t], acc[s][1]);
                }
#pragma unroll
                for (int i = 0; i < 8; ++i) { __builtin_amdgcn_sched_group_barrier(0x8, 1, 0); __builtin_amdgcn_sched_group_barrier(0x100, 1, 0); }
            }
            SB();
        }
    }
    DRAIN();
    BAR();
    ST(11);
    const int fb = sl * 8 + wave;
    u32x4 W2[7][4];
    {
        W2RD(0, ew2); W2RD(1, ha2);
        f32x4 tt[2][2];
#pragma unroll
        for (int ty = 0; ty < 2; ++ty)
#pragma unroll
            for (int e = 0; e < 2; ++e) tt[ty][e] = *(const f32x4*)(smem + L_TT + ty * 1024 + (16 * wave + 4 * g + 2 * e) * 8);
        W2DMA(2, hb2);
        WAITL(); SB();
#pragma unroll
        for (int sp = 0; sp < 4; ++sp) {
            u32 o[4];
#pragma unroll
            for (int h = 0; h < 2; ++h) {
                const int s = 2 * sp + h;
                const f32x2 st = *(const f32x2*)(smem + L_ST2 + (s * 16 + n) * 8);
                float a[4];
#pragma unroll
                for (int r = 0; r < 4; ++r) {
                    const float gv = st.x * acc[s][0][r] + (st.y * tt[0][r >> 1][(r & 1) * 2] + tt[0][r >> 1][(r & 1) * 2 + 1]);
                    const float uv = st.x * acc[s][1][r] + (st.y * tt[1][r >> 1][(r & 1) * 2] + tt[1][r >> 1][(r & 1) * 2 + 1]);
                    a[r] = gv * uv * __builtin_amdgcn_rcpf(1.f + __builtin_amdgcn_exp2f(gv * -1.44269504f));
                }
                o[h * 2] = pk2(a[0], a[1]); o[h * 2 + 1] = pk2(a[2], a[3]);
            }
            const auto e0 = __builtin_amdgcn_permlane16_swap(o[0], o[2], false, false), e1 = __builtin_amdgcn_permlane16_swap(o[1], o[3], false, false);
            __builtin_amdgcn_raw_buffer_store_b128((u32x4){e0[0], e1[0], e0[1], e1[1]}, rsrc, OFF_ACT + ((rt * 8 + sp * 2 + (g & 1)) * 56 + (fb >> 1)) * 1024 + (((fb & 1) * 2 + (g >> 1)) * 16 + n) * 16, 0, 16);
            SB();
            if (sp == 0) W2DMA(3, ew2);
            if (sp == 1) W2DMA(4, ha2);
            if (sp == 2) { WAITV(11); W2RD(2, hb2); WAITL(); SB(); W2DMA(5, hb2); }
            SB();
        }
        ST(12);
        WAITV(11);
        W2RD(3, ew2); WAITL(); SB();
        W2DMA(6, ew2);
        WAITV(4);
        BAR();
        if (tid == 0) __hip_atomic_store(flg + (LN_FACT + rt) * 32 + sl, tag, RLX_AGENT);
    }
    if (wave == 1) { const float gv = poll_granule(modg + 448 + db * 64 + lane, tag); *(float*)(smem + L_MISC + 256 + lane * 4) = gv; }
    W2RD(4, ha2); W2RD(5, hb2);
    DRAIN();
    W2RD(6, ew2);
    if (wave == 0) poll_eq(flg + (LN_FACT + (rb >> 1)) * 32 + (lane < 14 ? lane : 0), tag);
    BAR();
    ST(13);
#define G2D 10
    u32x4 Af[14][2];
    const u32 as0 = OFF_ACT + (rb * 4 * 56 + wave * 7) * 1024;
#define ALD(i) do { BLD_SC1(Af[i][0], l16, as0 + (((i) / 7) * 2) * 57344 + (((i) % 7) >> 2) * 4096, (((i) % 7) & 3) * 1024); \
                    BLD_SC1(Af[i][1], l16, as0 + (((i) / 7) * 2 + 1) * 57344 + (((i) % 7) >> 2) * 4096, (((i) % 7) & 3) * 1024); } while (0)
#pragma unroll
    for (int i = 0; i < G2D; ++i) ALD(i);
    f32x4* red = (f32x4*)smem;
    f32x4 cc[2][4];
#define G2STEP(i) do { \
        if ((i) % 7 == 0) { _Pragma("unroll") for (int t = 0; t < 4; ++t) { cc[0][t] = (f32x4){0, 0, 0, 0}; cc[1][t] = (f32x4){0, 0, 0, 0}; } } \
        waitv2<2 * (((i) + G2D < 14 ? (i) + G2D : 14) - 1 - (i))>(Af[i][0], Af[i][1]); \
        _Pragma("unroll") for (int t = 0; t < 4; ++t) { cc[0][t] = mfma16(W2[(i) % 7][t], Af[i][0], cc[0][t]); cc[1][t] = mfma16(W2[(i) % 7][t], Af[i][1], cc[1][t]); } \
        SB(); \
        if ((i) + G2D < 14) ALD((i) + G2D < 14 ? (i) + G2D : 0); \
        if ((i) % 7 == 6) { _Pragma("unroll") for (int h = 0; h < 2; ++h) _Pragma("unroll") for (int t = 0; t < 4; ++t) red[((((i) / 7 * 2 + h) * 4 + t) * 8 + wave) * 64 + lane] = cc[h][t]; } \
        SB(); } while (0)
    G2STEP(0); G2STEP(1); G2STEP(2); G2STEP(3); G2STEP(4); G2STEP(5); G2STEP(6);
    G2STEP(7); G2STEP(8); G2STEP(9); G2STEP(10); G2STEP(11); G2STEP(12); G2STEP(13);
    ST(14);
    BAR();
    {
        const int srow = rb * 64 + (wave >> 1) * 16 + n, dl = 32 * (wave & 1) + 8 * g;
        const f32x4* rp = red + (wave * 2 + (g >> 1)) * 512 + (g & 1) * 32 + n;
        f32x4 v0 = {0, 0, 0, 0}, v1 = {0, 0, 0, 0};
#pragma unroll
        for (int w = 0; w < 8; ++w) { v0 += rp[w * 64]; v1 += rp[w * 64 + 16]; }
        const char* xp = smem + L_X1T + (dl >> 3) * 1024 + (lane & 3) * 8;
        const int st_ = wave >> 1, qq = n >> 2;
        const u32x2 xa = trread(xp + qq * 128 + ((st_ ^ (qq >> 1)) * 32)), xb = trread(xp + (4 + qq) * 128 + ((st_ ^ (2 + (qq >> 1))) * 32));
        const f32x4 g0 = *(const f32x4*)(smem + L_MISC + 256 + dl * 4), g1 = *(const f32x4*)(smem + L_MISC + 256 + dl * 4 + 16);
        const f32x2 o0 = up2(xa.x) + rbf2(rbf2((f32x2){v0.x, v0.y}) * (f32x2){g0.x, g0.y}), o1 = up2(xa.y) + rbf2(rbf2((f32x2){v0.z, v0.w}) * (f32x2){g0.z, g0.w});
        const f32x2 o2 = up2(xb.x) + rbf2(rbf2((f32x2){v1.x, v1.y}) * (f32x2){g1.x, g1.y}), o3 = up2(xb.y) + rbf2(rbf2((f32x2){v1.z, v1.w}) * (f32x2){g1.z, g1.w});
        *(u32x4*)(out + (size_t)srow * D + db * 64 + dl) = (u32x4){pk2(o0.x, o0.y), pk2(o1.x, o1.y), pk2(o2.x, o2.y), pk2(o3.x, o3.y)};
    }
    ST(15);
    ST_FLUSH();
}

extern "C" void kernel_launch(void* const* d_in, const int* in_sizes, int n_in,
                              void* d_out, int out_size, void* d_ws, size_t ws_size,
                              hipStream_t stream) {
    (void)hipFuncSetAttribute((const void*)fused, hipFuncAttributeMaxDynamicSharedMemorySize, LDS_BYTES);
    fused<<<256, 512, LDS_BYTES, stream>>>((const u16*)d_in[0], (const u16*)d_in[1], (const u16*)d_in[2], (const u16*)d_in[3], (const u16*)d_in[4], (const u16*)d_in[5],
                                           (const u16*)d_in[7], (const u16*)d_in[8], (const u16*)d_in[9], (const u16*)d_in[10], (const u16*)d_in[11], (char*)d_ws + ((ws_size / 2) & ~(size_t)4095), (u16*)d_out);
}
```

```cpp
#include <hip/hip_runtime.h>
#include <stdint.h>

typedef unsigned short u16;
typedef unsigned int u32;
typedef unsigned long long u64;
typedef __attribute__((ext_vector_type(8))) __bf16 bf16x8;
typedef __attribute__((ext_vector_type(2))) __bf16 bf16x2;
typedef __attribute__((ext_vector_type(4))) float f32x4;
typedef __attribute__((ext_vector_type(2))) float f32x2;
typedef __attribute__((ext_vector_type(4))) u32 u32x4;
typedef __attribute__((ext_vector_type(2))) u32 u32x2;
typedef __attribute__((ext_vector_type(4))) short s16x4;
#define LAS __attribute__((address_space(3)))
#define GAS __attribute__((address_space(1)))
typedef GAS u32 gu32;
typedef GAS u64 gu64;
#define DI __device__ __forceinline__
#define RLX_AGENT __ATOMIC_RELAXED, __HIP_MEMORY_SCOPE_AGENT
#if defined(__HIP_DEVICE_COMPILE__)
#define NOPK_ATTR __attribute__((target("no-packed-fp32-ops")))
#else
#define NOPK_ATTR
#endif
#define U2F(x) __builtin_bit_cast(float, (u32)(x))
#define F2U(x) __builtin_bit_cast(u32, (float)(x))
#define ALL(c) (__builtin_amdgcn_ballot_w64(!(c)) == 0)

static constexpr int S = 2048, D = 448, F = 1792;

DI float lo2f(u32 v) { return U2F(v << 16); }
DI float hi2f(u32 v) { return U2F(v & 0xffff0000u); }
DI float bf2f(u16 v) { return U2F(((u32)v) << 16); }
DI u32 pk2(float a, float b) { return __builtin_bit_cast(u32, __builtin_convertvector((f32x2){a, b}, bf16x2)); }
DI float rbf(float a) { return U2F(pk2(0.f, a)); }
DI f32x4 mfma16(u32x4 a, u32x4 b, f32x4 c) {
    return __builtin_amdgcn_mfma_f32_16x16x32_bf16(__builtin_bit_cast(bf16x8, a), __builtin_bit_cast(bf16x8, b), c, 0, 0, 0);
}
DI u32x2 trread(const char* p) { return __builtin_bit_cast(u32x2, __builtin_amdgcn_ds_read_tr16_b64_v4i16((LAS s16x4*)p)); }
DI u32x2 trread(const LAS char* p) { return __builtin_bit_cast(u32x2, __builtin_amdgcn_ds_read_tr16_b64_v4i16((LAS s16x4*)p)); }
template <int CTRL> DI float dpp_add(float v) { return v + __builtin_bit_cast(float, __builtin_amdgcn_update_dpp(0, __builtin_bit_cast(int, v), CTRL, 0xf, 0xf, true)); }
DI float swap16_add(float v) { auto r = __builtin_amdgcn_permlane16_swap(F2U(v), F2U(v), false, false); return U2F(r[0]) + U2F(r[1]); }
DI float swap32_add(float v) { auto r = __builtin_amdgcn_permlane32_swap(F2U(v), F2U(v), false, false); return U2F(r[0]) + U2F(r[1]); }
DI float sum_hi3(float v) { v = dpp_add<0x128>(v); v = swap16_add(v); return swap32_add(v); }

static constexpr u32 OFF_FKV = 4096;
static constexpr u32 OFF_FGL = OFF_FKV + 7 * 16384;
static constexpr u32 OFF_FH2 = OFF_FGL + 8 * 56 * 256;
static constexpr u32 OFF_FACT = OFF_FH2 + 2 * 16 * 1024;
static constexpr u32 OFF_FMOD = OFF_FACT + 48 * 1024;
static constexpr u32 OFF_FW2 = OFF_FMOD + 4096;
static constexpr u32 OFF_MODV = OFF_FW2 + 16384;
static constexpr u32 OFF_MODP = OFF_MODV + 8192;
static constexpr u32 OFF_TU = OFF_MODP + 7 * 6 * 8 * 64 * 8;
static constexpr u32 OFF_YT = OFF_TU + S * D * 2;
static constexpr u32 OFF_X1 = OFF_YT + S * D * 2;
static constexpr u32 OFF_H2 = OFF_X1 + S * D * 2;
static constexpr u32 OFF_ACT = OFF_H2 + S * D * 2;
static constexpr u32 OFF_W2T = OFF_ACT + 64 * 112 * 1024;
static constexpr u32 WS_END = OFF_W2T + 7 * 56 * 4096;

static constexpr int KFROW = 4160, UROW = 286 * 16;
static constexpr int L_KF = 0, L_RED = 0;
static constexpr int L_P0 = 40960;
static constexpr int L_U = 147456, L_YS = L_U;
static constexpr int L_MP = L_U + 2 * UROW;
static constexpr int L_SP = L_MP + 1024;
static constexpr int L_JUNK = 163840 - 2048;
static constexpr int L_MISC = 163840 - 1024;
static constexpr int L_E = 114688;
static constexpr int LDS_BYTES = 163840;
static_assert(L_SP + 1024 <= L_JUNK, "lds");

#define SB() __builtin_amdgcn_sched_barrier(0)
#define BAR() do { asm volatile("s_waitcnt lgkmcnt(0)" ::: "memory"); __builtin_amdgcn_s_barrier(); asm volatile("" ::: "memory"); } while (0)
#define DRAIN() asm volatile("s_waitcnt vmcnt(0)" ::: "memory")
#define WAITV(n) asm volatile("s_waitcnt vmcnt(%0)" :: "i"(n) : "memory")
#define WAITL() asm volatile("s_waitcnt lgkmcnt(0)" ::: "memory")
#define BLD(dst, SRD, voff, soff, imm) asm volatile("buffer_load_dwordx4 %0, %1, %2, %3 offen offset:%4" : "=v"(dst) : "v"(voff), "s"(SRD), "s"(soff), "i"(imm))
#define BLD_SC1(dst, voff, soff, imm) asm volatile("buffer_load_dwordx4 %0, %1, %2, %3 offen offset:%4 sc1" : "=v"(dst) : "v"(voff), "s"(srd), "s"(soff), "i"(imm))
#define BLD2(dst, SRD, voff, soff) asm volatile("buffer_load_dwordx2 %0, %1, %2, %3 offen" : "=v"(dst) : "v"(voff), "s"(SRD), "s"(soff))
#define BLDU(dst, SRD, voff, imm) asm volatile("buffer_load_ushort %0, %1, %2, 0 offen offset:%3" : "=v"(dst) : "v"(voff), "s"(SRD), "i"(imm))
#define BLD1_SC1(dst, voff) asm volatile("buffer_load_dword %0, %1, %2, 0 offen sc1" : "=v"(dst) : "v"(voff), "s"(srd))
#define DMA(SRD, m0v, voff, soff) asm volatile("s_mov_b32 m0, %0\n\ts_nop 0\n\tbuffer_load_dwordx4 %1, %2, %3 offen lds" :: "s"(m0v), "v"(voff), "s"(SRD), "s"(soff) : "memory")
#define ST16(data, voff) asm volatile("buffer_store_dwordx4 %0, %1, %2, 0 offen sc1\n\ts_nop 7" :: "v"(data), "v"(voff), "s"(srd) : "memory")
#define ST16S(data, voff, soff) asm volatile("buffer_store_dwordx4 %0, %1, %2, %3 offen sc1\n\ts_nop 3" :: "v"(data), "v"(voff), "s"(srd), "s"(soff) : "memory")
#define TOUCH(SRD, voff) asm volatile("s_mov_b32 m0, %0\n\ts_nop 0\n\tbuffer_load_dword %1, %2, 0 offen lds" :: "s"(lds0 + L_JUNK), "v"(voff), "s"(SRD) : "memory")
#define TOUCHS(SRD, voff, soff) asm volatile("s_mov_b32 m0, %0\n\ts_nop 0\n\tbuffer_load_dword %1, %2, %3 offen sc1 lds" :: "s"(lds0 + L_JUNK), "v"(voff), "s"(SRD), "s"(soff) : "memory")
#define DMA_SC1(m0v, voff, soff) asm volatile("s_mov_b32 m0, %0\n\ts_nop 0\n\tbuffer_load_dwordx4 %1, %2, %3 offen sc1 lds" :: "s"(m0v), "v"(voff), "s"(srd), "s"(soff) : "memory")
template <int N> DI void waitv1(u32x4& a) { asm volatile("s_waitcnt vmcnt(%1)" : "+v"(a) : "i"(N)); }
template <int N> DI void waitv2(u32x4& a, u32x4& b) { asm volatile("s_waitcnt vmcnt(%2)" : "+v"(a), "+v"(b) : "i"(N)); }
DI u32x4 mksrd(const void* p, u32 bytes) { return (u32x4){(u32)(size_t)p, (u32)((size_t)p >> 32), bytes, 0x00020000u}; }
static constexpr u32 SPIN_CAP = 1u << 18;

DI void poll4(u32x4 srd, u32 off, u32 tag) {
#pragma unroll 1
    for (u32 spins = 0; spins < SPIN_CAP; ++spins) {
        u32x4 f;
        asm volatile("buffer_load_dwordx4 %0, %1, %2, 0 offen sc1\n\ts_waitcnt vmcnt(0)" : "=v"(f) : "v"(off), "s"(srd) : "memory");
        if (ALL(f.x == tag && f.y == tag && f.z == tag && f.w == tag)) break;
        __builtin_amdgcn_s_sleep(2);
    }
}
#define FLAG(off) __hip_atomic_store((gu32*)(ws + (off)), tag, RLX_AGENT)
DI float sum_lo3(float v) { v = dpp_add<0xB1>(v); v = dpp_add<0x4E>(v); return dpp_add<0x141>(v); }
DI float silu_bf(float cv) { return rbf(cv * __builtin_amdgcn_rcpf(1.f + __builtin_amdgcn_exp2f(cv * -1.44269504f))); }

DI u32 row_stats(u32x4 xr, int lane) {
    float e[8];
#pragma unroll
    for (int jx = 0; jx < 4; ++jx) { e[2 * jx] = lo2f(xr[jx]); e[2 * jx + 1] = hi2f(xr[jx]); }
    float s = ((e[0] + e[1]) + (e[2] + e[3])) + ((e[4] + e[5]) + (e[6] + e[7]));
    if (lane >= 56) s = 0.f;
    const float m = sum_hi3(sum_lo3(s)) * (1.f / D);
    float qv = 0.f;
#pragma unroll
    for (int jx = 0; jx < 8; ++jx) qv += (e[jx] - m) * (e[jx] - m);
    if (lane >= 56) qv = 0.f;
    return pk2(m, __builtin_amdgcn_sqrtf(rbf(rbf(sum_hi3(sum_lo3(qv)) * (1.f / D)) + 1e-5f)));
}
DI float sum8_parts(gu64* p, u32 tag) {
    u64 pp[8];
#pragma unroll 1
    for (u32 spins = 0; spins < SPIN_CAP; ++spins) {
        bool ok = true;
#pragma unroll
        for (int i = 0; i < 8; ++i) { pp[i] = __hip_atomic_load(p + i * 64, RLX_AGENT); ok &= (u32)(pp[i] >> 32) == tag; }
        if (ALL(ok)) break;
        __builtin_amdgcn_s_sleep(4);
    }
#define PV(i) U2F((u32)pp[i])
    return rbf(((PV(0) + PV(1)) + (PV(2) + PV(3))) + ((PV(4) + PV(5)) + (PV(6) + PV(7))));
}
DI void lds_wait(volatile LAS u32* p, u32 want) {
#pragma unroll 1
    for (u32 spins = 0; spins < (SPIN_CAP << 4) && *p != want; ++spins) { }
    asm volatile("" ::: "memory");
}
#define LDS_ADD(p) __hip_atomic_fetch_add((LAS u32*)(p), 1u, __ATOMIC_RELAXED, __HIP_MEMORY_SCOPE_WORKGROUP)
constexpr int conv_tile(int tg, int i) { return tg ? (i == 0 ? 1 : i == 1 ? 2 : i == 2 ? 5 : 6) : (i == 0 ? 0 : i == 1 ? 3 : i == 2 ? 4 : 7); }
constexpr int conv_cnt(int tg, int x, int i) {
    const int np = tg ? 7 : 8; int c = 0;
    for (int xx = 0; xx <= x; ++xx) for (int ii = 0; ii < 4; ++ii) { if (xx == x && ii >= i) break; if (xx % np <= conv_tile(tg, ii)) ++c; }
    return c;
}
static constexpr int CPD = 6;
static constexpr int R0N = 3, R1H = 3, R1N = 6, TSKIP = 3, AUXDELAY = 5, TPACE = 16;
template <int TG, int X, int I>
DI void conv_mm(const u32x4 (&A)[4][8], u32x4 (&B)[CPD + 1], f32x4 (&acc)[4], const u32x4& ur, float Dv, float (&ud)[8]) {
    constexpr int NP = TG ? 7 : 8, T = conv_tile(TG, I), cnt = conv_cnt(TG, X, I);
    if constexpr (X % NP <= T) {
        acc[I] = mfma16(A[X / NP][T - X % NP], B[X % (CPD + 1)], acc[I]);
        if constexpr (cnt >= 40 && cnt < 48) { constexpr int e = cnt - 40; ud[e] = rbf(((e & 1) ? lo2f(ur[3 - (e >> 1)]) : hi2f(ur[3 - (e >> 1)])) * Dv); asm volatile("" : "+v"(ud[e])); SB(); }
        if constexpr ((cnt & 3) == 3) SB();
    }
}
template <int TG, int X>
DI void conv_step(const u32x4 (&A)[4][8], const char* bbase, u32x4 (&B)[CPD + 1], f32x4 (&acc)[4], const u32x4& ur, float Dv, float (&ud)[8]) {
    constexpr int NP = TG ? 7 : 8, Y = X + CPD;
    if constexpr (Y < 4 * NP) B[Y % (CPD + 1)] = *(const u32x4*)(bbase + 192 - 64 * (Y / NP) + 512 * (Y % NP));
    if constexpr (X >= 0) { conv_mm<TG, X, 0>(A, B, acc, ur, Dv, ud); conv_mm<TG, X, 1>(A, B, acc, ur, Dv, ud); conv_mm<TG, X, 2>(A, B, acc, ur, Dv, ud); conv_mm<TG, X, 3>(A, B, acc, ur, Dv, ud); }
    if constexpr (X + 1 < 4 * NP) conv_step<TG, X + 1>(A, bbase, B, acc, ur, Dv, ud);
}
template <int TG>
DI void conv_main(const u32x4 (&A)[4][8], const char* bbase, f32x4 (&acc)[4], const u32x4& ur, float Dv, float (&ud)[8]) {
    u32x4 B[CPD + 1];
    conv_step<TG, -CPD>(A, bbase, B, acc, ur, Dv, ud);
    SB();
}

#define QP(v, c) __builtin_bit_cast(float, __builtin_amdgcn_update_dpp(0, __builtin_bit_cast(int, v), c, 0xf, 0xf, true))
NOPK_ATTR __global__ __launch_bounds__(512, 2) void fused(const u16* __restrict__ q, const u16* __restrict__ k, const u16* __restrict__ v,
                                                const u16* __restrict__ shk, const u16* __restrict__ ssmk, const u16* __restrict__ ssmD,
                                                const u16* __restrict__ wg, const u16* __restrict__ wu, const u16* __restrict__ wd,
                                                const u16* __restrict__ c, const u16* __restrict__ wmod, char* __restrict__ ws, u16* __restrict__ out) {
    extern __shared__ __attribute__((aligned(16))) char smem[];
    const int b = __builtin_amdgcn_workgroup_id_x(), tid = __builtin_amdgcn_workitem_id_x(), lane = tid & 63;
    const int wave = __builtin_amdgcn_readfirstlane(tid >> 6);
    const char* pc0;
    asm volatile("s_getpc_b64 %0" : "=s"(pc0));
    asm volatile("" :: "s"(q), "s"(k), "s"(v), "s"(shk), "s"(ssmk), "s"(ssmD), "s"(wg), "s"(wu), "s"(wd), "s"(c), "s"(wmod), "s"(ws), "s"(out));
    gu64* modp = (gu64*)(ws + OFF_MODP);
    const u32x4 srd = mksrd(ws, WS_END);
    const int n = lane & 15, g = lane >> 4;
    const u32 lds0 = (u32)(size_t)(LAS char*)smem;
    const u32 l16 = lane * 16;
    volatile LAS u32* lcnt = (volatile LAS u32*)(smem + L_MISC);
    const bool mainwg = b < 224;
    const int xc = b & 7, ii = b >> 3;
    const int rt = 4 * (xc >> 1) + (ii & 3), sl = 7 * (xc & 1) + (ii >> 2);
    const int rb = 8 * (xc >> 1) + 4 * (xc & 1) + (ii & 3), db = ii >> 2;
    const int ro = xc * 32 + ii;
    const u32x4 srdg = mksrd(wg, D * F * 2), srdu = mksrd(wu, D * F * 2), srdq = mksrd(q, S * D * 2);
    const u32 tl_row = lane >> 3, tl_st = (((lane >> 1) & 3) ^ (lane >> 4)) * 32 + (lane & 1) * 16;
    const u32 w1voff = tl_row * (F * 2) + tl_st;
    const u32x4 srdw = wave < 4 ? srdg : srdu;
    const u32 w1m = lds0 + ((wave >> 2) * 8 + (wave & 3)) * 1024, w1s = (8 * (wave & 3)) * (F * 2) + sl * 256;
#define W1DMA(ks, slot) do { DMA(srdw, w1m + (slot) * 16384, w1voff, w1s + 32 * (ks) * (F * 2)); DMA(srdw, w1m + (slot) * 16384 + 4096, w1voff, w1s + 32 * (ks) * (F * 2) + 128); } while (0)
    u32 epoch, tag;
    const int wv = wave < 7 ? wave : 6, s8 = lane & 7;
    u32x4 xw, xrow, fm;
    const u32 fmo = OFF_FMOD + xc * 512 + (lane & 1) * 256;
#define XLOADS() do { BLD(xw, srdq, (u32)(s8 * (D * 2) + wv * 128 + (lane >> 3) * 16), ro * 8 * (D * 2), 0); BLD(xrow, srdq, (u32)((lane < 56 ? lane : 55) * 16), (ro * 8 + wave) * (D * 2), 0); } while (0)
#define XSTATS2() do { asm volatile("" : "+v"(xw), "+v"(xrow)); const u32 rs1 = row_stats(xrow, lane); if (lane == 0) *(u32*)(smem + L_SP + 512 + wave * 4) = rs1; } while (0)
#define XSTATS() do { asm volatile("s_waitcnt vmcnt(0)" : "+v"(xw), "+v"(xrow) :: "memory"); const u32 rs1 = row_stats(xrow, lane); if (lane == 0) *(u32*)(smem + L_SP + 512 + wave * 4) = rs1; } while (0)
#define SET_TAG() tag = __builtin_amdgcn_readfirstlane(epoch) * 0x9E3779B1u + 0x7F4A7C15u

    if (__builtin_expect(mainwg, 1)) {
        const int gq = b >> 5, j = b & 31;
        const int ip = (j & 7) * 4 + (j >> 3), d0 = 64 * gq + 2 * ip;
        const int ch = wave >> 2, rh = (wave >> 1) & 1, tg = wave & 1, w4 = wave & 3;
        const u32x4 srdk = mksrd(k, S * D * 2), srdv = mksrd(v, S * D * 2), srdf = mksrd(ssmk, S * D * 2),
                    srdm = mksrd(wmod, D * 6 * D * 2), srdc = mksrd(c, D * 2), srds = mksrd(shk, D * 8), srdD = mksrd(ssmD, D * 2);
        const u32 p0l = lds0 + L_P0 + wave * 3072;
        const char* p0r = smem + L_P0 + wave * 3072 + (n >> 2) * 128 + g * 32 + (lane & 3) * 8;
        const u32 tvo = (64 * j + 8 * wave + (lane >> 3)) * (D * 2) + gq * 128 + (lane & 7) * 16;
        const int kc = tid & 255;
        u32x4 kw; u32x2 pv, sw; u32 dvr;
        DMA(srdk, p0l + 1024, tvo, 0);
        DMA(srdv, p0l + 2048, tvo, 0);
        if (lane >= 32) DMA(srdk, p0l, tvo - 8 * D * 2, 0);
        BLD2(sw, srds, (u32)(lane * 8), gq * 512);
        BLD1_SC1(epoch, 0u * lane);
        BLDU(dvr, srdD, (u32)((d0 + ((lane >> 2) & 1)) * 2), 0);
        if (wave == 4) { const u32x4 srdp = mksrd(pc0, 26624); TOUCH(srdp, (u32)((ii * 64 + lane) * 16)); }
        if (wave == 5) TOUCHS(srd, lane < 56 ? OFF_FGL + (xc * 56 + lane) * 256 : OFF_FKV + gq * 16384 + (xc * 8 + lane - 56) * 256, 0);
        if (wave == 6) TOUCHS(srd, lane < 4 ? OFF_FH2 + (xc & 1) * 16384 + rt * 1024 + lane * 256 : lane < 8 ? OFF_FACT + (rb >> 1) * 1024 + (lane & 3) * 256 : lane < 12 ? OFF_FACT + 32768 + (rb >> 1) * 1024 + (lane & 3) * 256
                                   : lane < 14 ? OFF_FMOD + xc * 512 + (lane & 1) * 256 : OFF_FW2 + (xc * 7 + (lane < 21 ? lane - 14 : 0)) * 256, 0);
        if (tid < 60) *(u32x4*)(smem + L_U + (tid / 30) * UROW + (tid % 30) * 16) = (u32x4){0, 0, 0, 0};
        if (tid >= 64 && tid < 80) *(u32x4*)(smem + L_KF + ((tid - 64) >> 1) * KFROW + (tid & 1) * 16) = (u32x4){0, 0, 0, 0};
        if (tid >= 128 && tid < 160) lcnt[tid & 31] = 0;
        asm volatile("s_waitcnt vmcnt(0)" : "+v"(sw), "+v"(epoch), "+v"(dvr) :: "memory");
        {
            const u32 ko = tid * 16, po = kc ? ko - 8 : ko;
            BLD(kw, srdf, ko, d0 * 4096, 0); BLD2(pv, srdf, po, d0 * 4096);
            const u32x2 ka = trread(p0r + 512), kb2 = trread(p0r + 1024), kc2 = trread(p0r + 1536), va = trread(p0r + 2048), vb = trread(p0r + 2560);
            const float kk[11] = {hi2f(ka.x), lo2f(ka.y), hi2f(ka.y), lo2f(kb2.x), hi2f(kb2.x), lo2f(kb2.y), hi2f(kb2.y), lo2f(kc2.x), hi2f(kc2.x), lo2f(kc2.y), hi2f(kc2.y)};
            const u32 vv[4] = {va.x, va.y, vb.x, vb.y};
            const float w0 = lo2f(sw.x), w1 = hi2f(sw.x), w2 = lo2f(sw.y), w3 = hi2f(sw.y);
            float s[8], t[8];
#pragma unroll
            for (int e = 0; e < 8; ++e) { s[e] = rbf(kk[e] * w3); t[e] = rbf(kk[e + 1] * w2); }
            SB();
#pragma unroll
            for (int e = 0; e < 8; ++e) { s[e] = rbf(s[e] + t[e]); t[e] = rbf(kk[e + 2] * w1); }
            SB();
#pragma unroll
            for (int e = 0; e < 8; ++e) { s[e] = rbf(s[e] + t[e]); t[e] = rbf(kk[e + 3] * w0); }
            SB();
#pragma unroll
            for (int e = 0; e < 8; ++e) s[e] = rbf(s[e] + t[e]) * ((e & 1) ? hi2f(vv[e >> 1]) : lo2f(vv[e >> 1]));
            const u32x4 o = {pk2(s[7], s[6]), pk2(s[5], s[4]), pk2(s[3], s[2]), pk2(s[1], s[0])};
            ST16(o, (u32)(OFF_TU + (gq * 256 + j * 8 + wave) * 1024 + l16));
            SB();
            WAITV(1);
            asm volatile("" : "+v"(kw), "+v"(pv));
            if (kc == 0) pv = (u32x2){0, 0};
            char* kd = smem + L_KF + ch * 4 * KFROW + 32 + kc * 16;
            const u32 s0 = __builtin_amdgcn_alignbit(pv.y, pv.x, 16), s1 = __builtin_amdgcn_alignbit(kw.x, pv.y, 16), s2 = __builtin_amdgcn_alignbit(kw.y, kw.x, 16),
                      s3 = __builtin_amdgcn_alignbit(kw.z, kw.y, 16), s4 = __builtin_amdgcn_alignbit(kw.w, kw.z, 16);
            *(u32x4*)(kd) = kw;
            *(u32x4*)(kd + KFROW) = (u32x4){s1, s2, s3, s4};
            *(u32x4*)(kd + 2 * KFROW) = (u32x4){pv.y, kw.x, kw.y, kw.z};
            *(u32x4*)(kd + 3 * KFROW) = (u32x4){s0, s1, s2, s3};
            DRAIN();
        }
        SET_TAG();
        BAR();
        if (tid < 8) FLAG(OFF_FKV + gq * 16384 + (tid * 8 + (j >> 2)) * 256 + (j & 3) * 4);
        const int cc = (tid >> 2) & 1, oc = (tid >> 3) * 4 + (tid & 3);
        u32 rpa = lds0 + L_RED + cc * 8192 + ((((2 * oc) ^ ((oc >> 2) & 7)) ^ (cc * 8)) * 16), rpb = rpa ^ 16, yo = OFF_YT + ((tid >> 3) * 448 + d0) * 64 + (tid & 7) * 16;
        u32 rw = lds0 + L_RED + rh * 16384 + ch * 8192 + (((4 * n + g) ^ (n >> 1)) ^ (ch * 8)) * 16, fgo = OFF_FGL + (lane * 56 + (b >> 2)) * 256 + (b & 3) * 4;
        u32 rw1 = rw + tg * 1024, rw2 = rw - tg * 1024;
        asm volatile("" : "+v"(rpa), "+v"(rpb), "+v"(yo), "+v"(rw1), "+v"(rw2), "+v"(fgo));
        u32x4 A[4][8];
        const char* abase = smem + L_KF + (ch * 4 + ((3 - n) & 3)) * KFROW + 56 + 8 * (n >> 2) - 16 * g + 64 * 4 * rh;
#define A_READS() _Pragma("unroll") for (int r = 0; r < 4; ++r) _Pragma("unroll") for (int p = 0; p < 8; ++p) { \
            const u32x2 l = *(const volatile LAS u32x2*)(abase + 64 * r + 512 * p), h = *(const volatile LAS u32x2*)(abase + 64 * r + 512 * p + 8); \
            A[r][p] = (u32x4){l.x, l.y, h.x, h.y}; }
        if (wave < 4) {
            A_READS();
            if (wave == 0) { poll4(srd, OFF_FKV + gq * 16384 + (xc * 8 + (lane & 7)) * 256, tag); lcnt[2] = 1; } else lds_wait(lcnt + 2, 1u);
            u32x4 kv0, kv1;
            const u32 vo = (tid >> 1) * 1024 + (tid & 1) * 16;
            BLD_SC1(kv0, vo, OFF_TU + gq * 262144 + ip * 32, 0);
            BLD_SC1(kv1, vo, OFF_TU + gq * 262144 + 131072 + ip * 32, 0);
            waitv2<0>(kv0, kv1);
            char* dst = smem + L_U + (tid & 1) * UROW + (30 + (tid >> 1)) * 16;
            *(u32x4*)dst = kv0; *(u32x4*)(dst + 2048) = kv1;
        } else {
            u32x4 mw[2]; u32 cr[2];
            const int qty = j >> 3, part = j & 7;
            const u32 mr0 = 56 * part + 8 * w4 + (lane >> 3), mr1 = w4 < 3 ? mr0 + 32 : mr0;
            const u32 mc = (qty * D + gq * 64) * 2;
            BLD(mw[0], srdm, mr0 * (6 * D * 2) + (lane & 7) * 16, mc, 0); BLD(mw[1], srdm, mr1 * (6 * D * 2) + (lane & 7) * 16, mc, 0);
            BLDU(cr[0], srdc, mr0 * 2, 0); BLDU(cr[1], srdc, mr1 * 2, 0);
#pragma unroll
            for (int i = 0; i < R0N * 4; ++i) {
                const u32 m0v = lds0 + 49152 + w4 * 1024 + i * 4096, so = w1s + (i >> 2) * 32 * (F * 2) + (i & 1) * 128;
                if ((i & 2) == 0) DMA(srdg, m0v, w1voff, so); else DMA(srdu, m0v, w1voff, so);
            }
            A_READS();
            WAITV(R0N * 4);
            asm volatile("" : "+v"(mw[0]), "+v"(mw[1]), "+v"(cr[0]), "+v"(cr[1]));
            const float sc0 = silu_bf(lo2f(cr[0])), sc1 = w4 < 3 ? silu_bf(lo2f(cr[1])) : 0.f;
            float mine = 0.f;
#pragma unroll
            for (int jx = 0; jx < 8; ++jx) {
                const float s = sum_hi3(sc0 * ((jx & 1) ? hi2f(mw[0][jx >> 1]) : lo2f(mw[0][jx >> 1])) + sc1 * ((jx & 1) ? hi2f(mw[1][jx >> 1]) : lo2f(mw[1][jx >> 1])));
                if ((lane >> 3) == jx) mine = s;
            }
            *(float*)(smem + L_MP + w4 * 256 + ((lane & 7) * 8 + (lane >> 3)) * 4) = mine;
            WAITL();
            u32 tk = 0;
            if (lane == 0) tk = LDS_ADD(lcnt + 4);
            if (__builtin_amdgcn_readfirstlane(tk) == 3u) {
                asm volatile("" ::: "memory");
                const float* mp = (const float*)(smem + L_MP) + lane;
                __hip_atomic_store(modp + ((gq * 6 + qty) * 8 + part) * 64 + lane, ((u64)tag << 32) | F2U((mp[0] + mp[64]) + (mp[128] + mp[192])), RLX_AGENT);
            }
        }
        BAR();
        XLOADS();
        if (wave >= 4) {
#pragma unroll
            for (int i = R0N * 4; i < R1H * 4; ++i) {
                const u32 m0v = lds0 + 49152 + w4 * 1024 + i * 4096, so = w1s + (i >> 2) * 32 * (F * 2) + (i & 1) * 128;
                if ((i & 2) == 0) DMA(srdg, m0v, w1voff, so); else DMA(srdu, m0v, w1voff, so);
            }
        }
        f32x4 acc[4] = {{0, 0, 0, 0}, {0, 0, 0, 0}, {0, 0, 0, 0}, {0, 0, 0, 0}};
        const char* bbase = smem + L_U + ch * UROW + (28 + 2 * n + g) * 16 - 64 * 4 * rh - 192;
        const u32x4 ur = *(const u32x4*)(smem + L_U + cc * UROW + (30 + oc) * 16);
        float ud[8];
        if (tg) conv_main<1>(A, bbase, acc, ur, lo2f(dvr), ud); else conv_main<0>(A, bbase, acc, ur, lo2f(dvr), ud);
        {
            LAS char *w1_ = (LAS char*)(size_t)rw1, *w2_ = (LAS char*)(size_t)rw2;
            *(LAS f32x4*)w1_ = acc[0]; *(LAS f32x4*)(w2_ + 3072) = acc[1]; *(LAS f32x4*)(w1_ + 4096) = acc[2]; *(LAS f32x4*)(w2_ + 7168) = acc[3];
        }
        if (wave < 4) {
#pragma unroll
            for (int i = R1H * 4; i < R1N * 4; ++i) {
                const u32 m0v = lds0 + 49152 + w4 * 1024 + i * 4096, so = w1s + (i >> 2) * 32 * (F * 2) + (i & 1) * 128;
                if ((i & 2) == 0) DMA(srdg, m0v, w1voff, so); else DMA(srdu, m0v, w1voff, so);
            }
        }
        BAR();
        {
            const LAS char *rp = (const LAS char*)(size_t)rpa, *rq = (const LAS char*)(size_t)rpb;
            const f32x4 a0 = *(const LAS f32x4*)rp, a1 = *(const LAS f32x4*)rq, b0 = *(const LAS f32x4*)(rp + 16384), b1 = *(const LAS f32x4*)(rq + 16384);
            float y[8];
#pragma unroll
            for (int e = 0; e < 8; ++e) y[e] = rbf(e < 4 ? a0[e & 3] + b0[e & 3] : a1[e & 3] + b1[e & 3]) + ud[e];
            const u32x4 o = {pk2(y[0], y[1]), pk2(y[2], y[3]), pk2(y[4], y[5]), pk2(y[6], y[7])};
            ST16(o, yo);
        }
        BLD_SC1(fm, fmo, 0, 0);
        if (wave >= 4) WAITV((R1H - R0N) * 4 + 2); else WAITV((R1N - R1H) * 4 + 2);
        XSTATS2();
        asm volatile("s_waitcnt vmcnt(0)" : "+v"(fm) :: "memory");
        BAR();
        if (tid < 8) FLAG(fgo);
    } else {
        const int a = b - 224;
        asm volatile("buffer_load_dword %0, %1, %2, 0 offen sc1\n\ts_waitcnt vmcnt(0)" : "=v"(epoch) : "v"(0u * lane), "s"(srd) : "memory");
        SET_TAG();
        if (tid < 32) lcnt[tid] = 0;
        __builtin_amdgcn_s_sleep(AUXDELAY);
        if (wave * 32 + a < 112) {
            const int task = wave * 32 + a, part = task & 7, blk = (task >> 3) % 7, qty = 4 + task / 56;
            u32x4 w[7]; u16 cr[7];
#pragma unroll
            for (int i = 0; i < 7; ++i) { const int r = 56 * part + 8 * i + (lane >> 3); cr[i] = c[r]; w[i] = *(const u32x4*)(wmod + (size_t)r * (6 * D) + qty * D + blk * 64 + (lane & 7) * 8); }
            float acc[8] = {0, 0, 0, 0, 0, 0, 0, 0};
#pragma unroll
            for (int i = 0; i < 7; ++i) {
                const float sc = silu_bf(bf2f(cr[i]));
#pragma unroll
                for (int jx = 0; jx < 4; ++jx) { acc[2 * jx] += sc * lo2f(w[i][jx]); acc[2 * jx + 1] += sc * hi2f(w[i][jx]); }
            }
            float mine = 0.f;
#pragma unroll
            for (int jx = 0; jx < 8; ++jx) { const float s = sum_hi3(acc[jx]); if ((lane >> 3) == jx) mine = s; }
            __hip_atomic_store(modp + ((blk * 6 + qty) * 8 + part) * 64 + (lane & 7) * 8 + (lane >> 3), ((u64)tag << 32) | F2U(mine), RLX_AGENT);
        }
#define W2FETCH() do { const u32x4 srdd = mksrd(wd, D * F * 2); \
            _Pragma("unroll") for (int i = 0; i < 8; ++i) DMA(srdd, lds0 + (wave * 8 + i) * 1024, tl_row * (D * 2) + tl_st, (a * 64 + i * 8) * (D * 2) + wv * 128); } while (0)
        if (a >= 25) {
            if (a < 28) W2FETCH();
            if (wave < 6) {
                float s = sum8_parts(modp + ((a - 25) * 6 + wave) * 512 + lane, tag);
                if (wave == 1 || wave == 4) s = rbf(1.f + s);
                const float o = QP(s, 0xB1);
                if ((lane & 1) == 0) __hip_atomic_store((gu32*)(ws + OFF_MODV + wave * 1024 + ((a - 25) * 64 + lane) * 2), pk2(s, o), RLX_AGENT);
                DRAIN();
            }
            BAR();
            if (tid < 16) FLAG(OFF_FMOD + (tid & 7) * 512 + ((a - 25) >> 2) * 256 + (((a - 25) & 3) + (a == 31 ? tid >> 3 : 0)) * 4);
        } else {
#pragma unroll 1
            for (int i = 0; i < 2 * ((1568 - TSKIP * 112 + 199) / 200); ++i) { const int pc = TSKIP * 112 + (i >> 1) * 200 + a * 8 + wave; if (pc < 1568) { if (i & 1) DMA(srdu, lds0 + L_JUNK, l16, pc * 1024); else DMA(srdg, lds0 + L_JUNK, l16, pc * 1024); } WAITV(TPACE); }
            BAR();
        }
        XLOADS();
        if (a < 25) W2FETCH();
        XSTATS();
        fm = (u32x4){~tag, 0, 0, 0};
        BAR();
    }

    u32x4 W[14][2];
    const char* wrb = smem + (wave >> 2) * 4096 + g * 1024 + (n >> 2) * 128 + (lane & 3) * 8;
    u32 wl_[3], wh_[3];
#pragma unroll
    for (int i = 0; i < 3; ++i) { wl_[i] = (u32)(size_t)(LAS char*)wrb + i * 49152 + ((wave & 3) ^ (n >> 3)) * 32; wh_[i] = (u32)(size_t)(LAS char*)wrb + i * 49152 + 512 + ((wave & 3) ^ (2 + (n >> 3))) * 32; asm volatile("" : "+v"(wl_[i]), "+v"(wh_[i])); }
#define W1RD(ks, slot) do { _Pragma("unroll") for (int ty = 0; ty < 2; ++ty) { \
        const u32x2 l_ = trread((const LAS char*)(size_t)wl_[(slot) / 3] + ((slot) % 3) * 16384 + ty * 8192), h_ = trread((const LAS char*)(size_t)wh_[(slot) / 3] + ((slot) % 3) * 16384 + ty * 8192); \
        W[ks][ty] = (u32x4){l_.x, l_.y, h_.x, h_.y}; } } while (0)
    {
        u32x4 mv[5];
        if (!ALL(fm.x == tag && fm.y == tag && fm.z == tag && fm.w == tag)) poll4(srd, fmo, tag);
#pragma unroll
        for (int i = 0; i < 5; ++i) BLD_SC1(mv[i], (u32)(wv * 128 + (lane >> 3) * 16), OFF_MODV + i * 1024, 0);
        if (mainwg) {
#pragma unroll
            for (int ks = R1N; ks < 9; ++ks) W1DMA(ks, ks < 6 ? ks + 3 : ks - 6);
#pragma unroll
            for (int ks = 0; ks < R1N; ++ks) W1RD(ks, ks + 3);
        }
        if (mainwg) WAITV(2 * (9 - R1N)); else DRAIN();
        asm volatile("" : "+v"(mv[0]), "+v"(mv[1]), "+v"(mv[2]), "+v"(mv[3]), "+v"(mv[4]));
#define EL(v, e) (((e) & 1) ? hi2f((v)[(e) >> 1]) : lo2f((v)[(e) >> 1]))
        float x[8], h[8], ga[8], cb[8], sb[8];
        u32 h2o = OFF_H2 + (((ro >> 2) * 14 + wave * 2 + (lane >> 5)) * 2 + ((ro >> 1) & 1)) * 1024 + (((lane >> 3) & 3) * 16 + (ro & 1) * 8 + s8) * 16;
        u32 ypo = lds0 + L_YS + wave * 1024 + ((2 * g + ((lane >> 1) & 1)) * 8 + (n >> 2)) * 16 + (lane & 1) * 8, spw = lds0 + L_SP + lane * 64 + wave * 8, spr = lds0 + L_SP + s8 * 64;
        u32 fho = OFF_FH2 + lane * 16384 + (ro >> 2) * 256 + (ro & 3) * 4;
        asm volatile("" : "+v"(h2o), "+v"(ypo), "+v"(spw), "+v"(spr), "+v"(fho));
        if (wave < 7) {
            const u32 st = *(const u32*)(smem + L_SP + 512 + s8 * 4);
            const float m = lo2f(st), rd = __builtin_amdgcn_rcpf(hi2f(st));
#pragma unroll
            for (int e = 0; e < 8; ++e) {
                x[e] = EL(xw, e); h[e] = rbf(rbf(rbf(rbf(x[e] - m) * rd) * EL(mv[1], e)) + EL(mv[0], e));
                ga[e] = EL(mv[2], e); cb[e] = EL(mv[4], e); sb[e] = EL(mv[3], e);
                asm volatile("" : "+v"(x[e]), "+v"(h[e]), "+v"(ga[e]), "+v"(cb[e]), "+v"(sb[e]));
            }
            SB();
            BAR();
            DMA_SC1(lds0 + L_YS + wave * 1024, (u32)(lane * 64), OFF_YT + ((ro >> 2) * 448 + wave * 64) * 64 + (ro & 3) * 16);
            DRAIN();
            const LAS char* yp = (const LAS char*)(size_t)ypo;
            const u32x2 ya = trread(yp), yb = trread(yp + 64);
            const u32x4 yw = {ya.x, ya.y, yb.x, yb.y};
#pragma unroll
            for (int e = 0; e < 8; ++e) h[e] = rbf(h[e] * EL(yw, e));
            SB();
#pragma unroll
            for (int e = 0; e < 8; ++e) h[e] = rbf(h[e] * ga[e]);
            SB();
#pragma unroll
            for (int e = 0; e < 8; ++e) x[e] = rbf(x[e] + h[e]);
            float s1 = ((x[0] + x[1]) + (x[2] + x[3])) + ((x[4] + x[5]) + (x[6] + x[7]));
            float s2 = ((x[0] * x[0] + x[1] * x[1]) + (x[2] * x[2] + x[3] * x[3])) + ((x[4] * x[4] + x[5] * x[5]) + (x[6] * x[6] + x[7] * x[7]));
            s1 = sum_hi3(s1); s2 = sum_hi3(s2);
            if (lane < 8) *(LAS f32x2*)(size_t)spw = (f32x2){s1, s2};
        } else {
            if (lane < 8) *(f32x2*)(smem + L_SP + lane * 64 + 56) = (f32x2){0.f, 0.f};
            poll4(srd, OFF_FGL + (xc * 56 + (lane < 56 ? lane : 0)) * 256, tag);
            BAR();
        }
        BAR();
        if (wave < 7) {
            const LAS f32x4* sp = (const LAS f32x4*)(size_t)spr;
            const f32x4 t = (sp[0] + sp[1]) + (sp[2] + sp[3]);
            const float m = (t.x + t.z) * (1.f / D);
            const float rd = __builtin_amdgcn_rcpf(rbf(__builtin_amdgcn_sqrtf(rbf(rbf(__builtin_fmaxf((t.y + t.w) * (1.f / D) - m * m, 0.f)) + 1e-5f))));
            const float mb = rbf(m);
#pragma unroll
            for (int e = 0; e < 8; ++e) h[e] = rbf(rbf(rbf(x[e] - mb) * rd) * cb[e]) + sb[e];
            const u32x4 ho = {pk2(h[0], h[1]), pk2(h[2], h[3]), pk2(h[4], h[5]), pk2(h[6], h[7])};
            ST16(ho, h2o);
            SB();
            const u32x4 xo = {__builtin_amdgcn_perm(F2U(x[1]), F2U(x[0]), 0x07060302u), __builtin_amdgcn_perm(F2U(x[3]), F2U(x[2]), 0x07060302u),
                              __builtin_amdgcn_perm(F2U(x[5]), F2U(x[4]), 0x07060302u), __builtin_amdgcn_perm(F2U(x[7]), F2U(x[6]), 0x07060302u)};
            ST16(xo, (u32)(OFF_X1 + (ro * 7 + wave) * 1024 + l16));
        }
        SB();
        if (mainwg) {
#pragma unroll
            for (int ks = R1N; ks < 9; ++ks) W1RD(ks, ks < 6 ? ks + 3 : ks - 6);
        }
        DRAIN();
        BAR();
        if (tid < 2) FLAG(fho);
    }
    if (!mainwg) {
        if (b < 224 + 28) {
            const int a = b - 224;
            DRAIN();
            if (wave < 7) {
#pragma unroll
                for (int r = 0; r < 2; ++r)
#pragma unroll
                    for (int t = 0; t < 4; ++t) {
                        const char* p_ = smem + (wave * 8 + r * 4 + g) * 1024 + (n >> 2) * 128 + (lane & 3) * 8;
                        const u32x2 l_ = trread(p_ + ((t ^ (n >> 3)) * 32)), h_ = trread(p_ + 512 + ((t ^ (2 + (n >> 3))) * 32));
                        const u32x4 d_ = {l_.x, l_.y, h_.x, h_.y};
                        ST16(d_, (u32)(OFF_W2T + ((wave * 56 + a * 2 + r) * 4 + t) * 1024 + l16));
                    }
            }
            DRAIN();
            BAR();
            if (tid < 8) FLAG(OFF_FW2 + (tid * 7 + (a >> 2)) * 256 + (a & 3) * 4);
        }
        if (b == 224 && wave == 0) {
            poll4(srd, OFF_FH2 + lane * 256, tag);
            if (lane == 0) __hip_atomic_store((gu32*)ws, tag, RLX_AGENT);
        }
        return;
    }

    f32x4 acc[8][2];
#pragma unroll
    for (int s = 0; s < 8; ++s) { acc[s][0] = (f32x4){0, 0, 0, 0}; acc[s][1] = (f32x4){0, 0, 0, 0}; }
    const int fb = sl * 8 + wave;
    const int kw2 = (wave + db) & 7;
    u32x4 B[2][4], W2[7][4], Af[28], x1w, gbw;
    u32 eo[4];
    f32x4 cc[4];
    float eg0[4], eg1[4], eg2[4], ee0[4], ee1[4], ee2[4];
    const u32 w2s = OFF_W2T + (db * 56 + kw2 * 7) * 4096;
    const u32 as0 = OFF_ACT + (((rb >> 1) * 8 + (rb & 1)) * 56 + kw2 * 7) * 1024;
    const u32 aso = OFF_ACT + ((rt * 8 + (g & 1)) * 56 + (fb >> 1)) * 1024 + (((fb & 1) * 2 + (g >> 1)) * 16 + n) * 16;
    const int srow = (rb >> 1) * 128 + (wave >> 1) * 32 + (rb & 1) * 16 + n, dl = 32 * (wave & 1) + 8 * g;
    const char* hb0 = smem + l16;
    u32 hb1_ = lds0 + l16 + 61440;
    asm volatile("" : "+v"(hb1_));
    const LAS char* hb1 = (const LAS char*)(size_t)hb1_;
    u32x4 fw2;
    const u32 fwo = OFF_FW2 + (xc * 7 + (lane < 7 ? lane : 0)) * 256;
#define FW2_CHECK(nn) do { if (wave == 1) { u32x4 f_; asm volatile("s_waitcnt vmcnt(%4)\n\tv_mov_b64 %0, %2\n\tv_mov_b64 %1, %3" : "=&v"(f_.lo), "=&v"(f_.hi) : "v"(fw2.lo), "v"(fw2.hi), "i"(nn) : "memory"); \
        if (!ALL(f_.x == tag && f_.y == tag && f_.z == tag && f_.w == tag)) poll4(srd, fwo, tag); } } while (0)
    const u32x4 srdo = mksrd(out, S * D * 2);
#define HDMA(i) DMA_SC1(lds0 + ((i) * 8 + wave) * 1024, l16, OFF_H2 + (rt * 112 + (i) * 8 + wave) * 1024)
#define EPI_STORE(sp) do { const auto e0_ = __builtin_amdgcn_permlane16_swap(eo[0], eo[2], false, false), e1_ = __builtin_amdgcn_permlane16_swap(eo[1], eo[3], false, false); \
        const u32x4 d_ = {e0_[0], e1_[0], e0_[1], e1_[1]}; ST16S(d_, aso, (sp) * 2 * 57344); } while (0)
#define ARRIVE(i) do { u32 tk_ = 0; if (lane == 0) tk_ = LDS_ADD(lcnt + 6 + (i)); \
        if (__builtin_amdgcn_readfirstlane(tk_) == 7u && lane < (sl >= 12 ? 2 : 1)) FLAG(OFF_FACT + (i) * 16384 + rt * 1024 + (sl >> 2) * 256 + ((sl & 3) + 2 * lane) * 4); } while (0)
#define WAIT_F(i) do { u32 tk_ = 0; if (lane == 0) tk_ = __hip_atomic_fetch_or((LAS u32*)lcnt + 10 + (i), 1u, __ATOMIC_RELAXED, __HIP_MEMORY_SCOPE_WORKGROUP); \
        if (__builtin_amdgcn_readfirstlane(tk_) == 0u) { poll4(srd, OFF_FACT + (i) * 16384 + (rb >> 1) * 1024 + (lane & 3) * 256, tag); lcnt[10 + (i)] = 3; } else lds_wait(lcnt + 10 + (i), 3u); } while (0)
#define STORED() do { if (lane == 0) LDS_ADD(lcnt + 9); } while (0)
#define CC_ZERO() do { _Pragma("unroll") for (int t = 0; t < 4; ++t) cc[t] = (f32x4){0, 0, 0, 0}; } while (0)
#define G2MM(i) do { _Pragma("unroll") for (int t = 0; t < 4; ++t) cc[t] = mfma16(W2[(i) % 7][t], Af[i], cc[t]); } while (0)
#define RED_OFF(T, w) ((T) < 14 ? (w) * 14336 + (T) * 1024 : L_E + (w) * 4096 + ((T) - 14) * 1024)
#define RED_WRITE(st) do { _Pragma("unroll") for (int t = 0; t < 4; ++t) *(f32x4*)(smem + RED_OFF((st) * 4 + t, wave) + l16) = cc[t]; } while (0)
    u32 ra[8], oo;
    float xf[8], gf[8];
#define FINPREP() do { const int T = wave * 2 + (g >> 1), rs_ = T < 14 ? 14336 : 4096; oo = srow * (D * 2) + db * 128 + dl * 2; \
        _Pragma("unroll") for (int w = 0; w < 8; ++w) { ra[w] = lds0 + RED_OFF(T, 0) + ((g & 1) * 32 + n) * 16 + w * rs_; xf[w] = EL(x1w, w); gf[w] = EL(gbw, w); asm volatile("" : "+v"(ra[w]), "+v"(xf[w]), "+v"(gf[w])); } \
        asm volatile("" : "+v"(oo)); SB(); } while (0)
    W1DMA(9, 3);
    W1DMA(10, 4);
    W1DMA(11, 5);
    W1DMA(12, 6);
    W1DMA(13, 7);
    if (wave == 0) poll4(srd, OFF_FH2 + (xc & 1) * 16384 + rt * 1024 + (lane & 3) * 256, tag);
    DRAIN();
    BAR();
    HDMA(0);
    HDMA(1);
    HDMA(2);
    HDMA(3);
    W1RD(9, 3);
    W1RD(10, 4);
    W1RD(11, 5);
    W1RD(12, 6);
    W1RD(13, 7);
    if (wave >= 4) __builtin_amdgcn_s_setprio(1);
    WAITV(3);
    BAR();
    HDMA(4);
    HDMA(5);
    SB();
    B[0][0] = *(const u32x4*)(hb0 + 0);
    SB();
    SB();
    B[0][1] = *(const u32x4*)(hb0 + 1024);
    SB();
    SB();
    B[0][2] = *(const u32x4*)(hb0 + 2048);
    SB();
    SB();
    B[0][3] = *(const u32x4*)(hb0 + 3072);
    SB();
    SB();
    HDMA(6);
    HDMA(7);
    SB();
    acc[0][0] = mfma16(W[0][0], B[0][0], acc[0][0]);
    SB();
    B[1][0] = *(const u32x4*)(hb0 + 4096);
    SB();
    acc[0][1] = mfma16(W[0][1], B[0][0], acc[0][1]);
    SB();
    SB();
    acc[1][0] = mfma16(W[0][0], B[0][1], acc[1][0]);
    SB();
    B[1][1] = *(const u32x4*)(hb0 + 5120);
    SB();
    acc[1][1] = mfma16(W[0][1], B[0][1], acc[1][1]);
    SB();
    SB();
    acc[0][0] = mfma16(W[1][0], B[0][2], acc[0][0]);
    SB();
    B[1][2] = *(const u32x4*)(hb0 + 6144);
    SB();
    acc[0][1] = mfma16(W[1][1], B[0][2], acc[0][1]);
    SB();
    SB();
    acc[1][0] = mfma16(W[1][0], B[0][3], acc[1][0]);
    SB();
    B[1][3] = *(const u32x4*)(hb0 + 7168);
    SB();
    acc[1][1] = mfma16(W[1][1], B[0][3], acc[1][1]);
    SB();
    SB();
    WAITV(4);
    BAR();
    if (wave == 1) BLD_SC1(fw2, fwo, 0, 0);
    HDMA(8);
    HDMA(9);
    SB();
    acc[0][0] = mfma16(W[2][0], B[1][0], acc[0][0]);
    SB();
    B[0][0] = *(const u32x4*)(hb0 + 8192);
    SB();
    acc[0][1] = mfma16(W[2][1], B[1][0], acc[0][1]);
    SB();
    SB();
    acc[1][0] = mfma16(W[2][0], B[1][1], acc[1][0]);
    SB();
    B[0][1] = *(const u32x4*)(hb0 + 9216);
    SB();
    acc[1][1] = mfma16(W[2][1], B[1][1], acc[1][1]);
    SB();
    SB();
    acc[0][0] = mfma16(W[3][0], B[1][2], acc[0][0]);
    SB();
    B[0][2] = *(const u32x4*)(hb0 + 10240);
    SB();
    acc[0][1] = mfma16(W[3][1], B[1][2], acc[0][1]);
    SB();
    SB();
    acc[1][0] = mfma16(W[3][0], B[1][3], acc[1][0]);
    SB();
    B[0][3] = *(const u32x4*)(hb0 + 11264);
    SB();
    acc[1][1] = mfma16(W[3][1], B[1][3], acc[1][1]);
    SB();
    SB();
    HDMA(10);
    HDMA(11);
    SB();
    acc[0][0] = mfma16(W[4][0], B[0][0], acc[0][0]);
    SB();
    B[1][0] = *(const u32x4*)(hb0 + 12288);
    SB();
    acc[0][1] = mfma16(W[4][1], B[0][0], acc[0][1]);
    SB();
    SB();
    acc[1][0] = mfma16(W[4][0], B[0][1], acc[1][0]);
    SB();
    B[1][1] = *(const u32x4*)(hb0 + 13312);
    SB();
    acc[1][1] = mfma16(W[4][1], B[0][1], acc[1][1]);
    SB();
    SB();
    acc[0][0] = mfma16(W[5][0], B[0][2], acc[0][0]);
    SB();
    B[1][2] = *(const u32x4*)(hb0 + 14336);
    SB();
    acc[0][1] = mfma16(W[5][1], B[0][2], acc[0][1]);
    SB();
    SB();
    acc[1][0] = mfma16(W[5][0], B[0][3], acc[1][0]);
    SB();
    B[1][3] = *(const u32x4*)(hb0 + 15360);
    SB();
    acc[1][1] = mfma16(W[5][1], B[0][3], acc[1][1]);
    SB();
    SB();
    HDMA(12);
    HDMA(13);
    SB();
    acc[0][0] = mfma16(W[6][0], B[1][0], acc[0][0]);
    SB();
    B[0][0] = *(const u32x4*)(hb0 + 16384);
    SB();
    acc[0][1] = mfma16(W[6][1], B[1][0], acc[0][1]);
    SB();
    SB();
    acc[1][0] = mfma16(W[6][0], B[1][1], acc[1][0]);
    SB();
    B[0][1] = *(const u32x4*)(hb0 + 17408);
    SB();
    acc[1][1] = mfma16(W[6][1], B[1][1], acc[1][1]);
    SB();
    SB();
    acc[0][0] = mfma16(W[7][0], B[1][2], acc[0][0]);
    SB();
    B[0][2] = *(const u32x4*)(hb0 + 18432);
    SB();
    acc[0][1] = mfma16(W[7][1], B[1][2], acc[0][1]);
    SB();
    SB();
    acc[1][0] = mfma16(W[7][0], B[1][3], acc[1][0]);
    SB();
    B[0][3] = *(const u32x4*)(hb0 + 19456);
    SB();
    acc[1][1] = mfma16(W[7][1], B[1][3], acc[1][1]);
    SB();
    SB();
    SB();
    acc[0][0] = mfma16(W[8][0], B[0][0], acc[0][0]);
    SB();
    B[1][0] = *(const u32x4*)(hb0 + 20480);
    SB();
    acc[0][1] = mfma16(W[8][1], B[0][0], acc[0][1]);
    SB();
    SB();
    acc[1][0] = mfma16(W[8][0], B[0][1], acc[1][0]);
    SB();
    B[1][1] = *(const u32x4*)(hb0 + 21504);
    SB();
    acc[1][1] = mfma16(W[8][1], B[0][1], acc[1][1]);
    SB();
    SB();
    acc[0][0] = mfma16(W[9][0], B[0][2], acc[0][0]);
    SB();
    B[1][2] = *(const u32x4*)(hb0 + 22528);
    SB();
    acc[0][1] = mfma16(W[9][1], B[0][2], acc[0][1]);
    SB();
    SB();
    acc[1][0] = mfma16(W[9][0], B[0][3], acc[1][0]);
    SB();
    B[1][3] = *(const u32x4*)(hb0 + 23552);
    SB();
    acc[1][1] = mfma16(W[9][1], B[0][3], acc[1][1]);
    SB();
    SB();
    BLD_SC1(gbw, (u32)(db * 128 + dl * 2), OFF_MODV + 5 * 1024, 0);
    SB();
    acc[0][0] = mfma16(W[10][0], B[1][0], acc[0][0]);
    SB();
    B[0][0] = *(const u32x4*)(hb0 + 24576);
    SB();
    acc[0][1] = mfma16(W[10][1], B[1][0], acc[0][1]);
    SB();
    SB();
    acc[1][0] = mfma16(W[10][0], B[1][1], acc[1][0]);
    SB();
    B[0][1] = *(const u32x4*)(hb0 + 25600);
    SB();
    acc[1][1] = mfma16(W[10][1], B[1][1], acc[1][1]);
    SB();
    SB();
    acc[0][0] = mfma16(W[11][0], B[1][2], acc[0][0]);
    SB();
    B[0][2] = *(const u32x4*)(hb0 + 26624);
    SB();
    acc[0][1] = mfma16(W[11][1], B[1][2], acc[0][1]);
    SB();
    SB();
    acc[1][0] = mfma16(W[11][0], B[1][3], acc[1][0]);
    SB();
    B[0][3] = *(const u32x4*)(hb0 + 27648);
    SB();
    acc[1][1] = mfma16(W[11][1], B[1][3], acc[1][1]);
    SB();
    SB();
    SB();
    acc[0][0] = mfma16(W[12][0], B[0][0], acc[0][0]);
    SB();
    B[1][0] = *(const u32x4*)(hb0 + 28672);
    SB();
    acc[0][1] = mfma16(W[12][1], B[0][0], acc[0][1]);
    SB();
    SB();
    acc[1][0] = mfma16(W[12][0], B[0][1], acc[1][0]);
    SB();
    B[1][1] = *(const u32x4*)(hb0 + 29696);
    SB();
    acc[1][1] = mfma16(W[12][1], B[0][1], acc[1][1]);
    SB();
    SB();
    acc[0][0] = mfma16(W[13][0], B[0][2], acc[0][0]);
    SB();
    B[1][2] = *(const u32x4*)(hb0 + 30720);
    SB();
    acc[0][1] = mfma16(W[13][1], B[0][2], acc[0][1]);
    SB();
    SB();
    acc[1][0] = mfma16(W[13][0], B[0][3], acc[1][0]);
    SB();
    B[1][3] = *(const u32x4*)(hb0 + 31744);
    SB();
    acc[1][1] = mfma16(W[13][1], B[0][3], acc[1][1]);
    SB();
    SB();
    FW2_CHECK(7);
    WAITV(6);
    BAR();
    SB();
    acc[2][0] = mfma16(W[0][0], B[1][0], acc[2][0]);
    SB();
    B[0][0] = *(const u32x4*)(hb0 + 32768);
    SB();
    acc[2][1] = mfma16(W[0][1], B[1][0], acc[2][1]);
    SB();
    SB();
    acc[3][0] = mfma16(W[0][0], B[1][1], acc[3][0]);
    SB();
    B[0][1] = *(const u32x4*)(hb0 + 33792);
    SB();
    acc[3][1] = mfma16(W[0][1], B[1][1], acc[3][1]);
    SB();
    SB();
    acc[2][0] = mfma16(W[1][0], B[1][2], acc[2][0]);
    SB();
    B[0][2] = *(const u32x4*)(hb0 + 34816);
    SB();
    acc[2][1] = mfma16(W[1][1], B[1][2], acc[2][1]);
    SB();
    SB();
    acc[3][0] = mfma16(W[1][0], B[1][3], acc[3][0]);
    SB();
    B[0][3] = *(const u32x4*)(hb0 + 35840);
    SB();
    acc[3][1] = mfma16(W[1][1], B[1][3], acc[3][1]);
    SB();
    SB();
    DMA_SC1(lds0 + L_E + wave * 4096 + 0, l16, w2s + 27648);
    SB();
    acc[2][0] = mfma16(W[2][0], B[0][0], acc[2][0]);
    SB();
    B[1][0] = *(const u32x4*)(hb0 + 36864);
    ee0[0] = acc[0][0][0] * -1.44269504f;
    ee0[1] = acc[0][0][1] * -1.44269504f;
    SB();
    acc[2][1] = mfma16(W[2][1], B[0][0], acc[2][1]);
    SB();
    ee0[2] = acc[0][0][2] * -1.44269504f;
    ee0[3] = acc[0][0][3] * -1.44269504f;
    SB();
    acc[3][0] = mfma16(W[2][0], B[0][1], acc[3][0]);
    SB();
    B[1][1] = *(const u32x4*)(hb0 + 37888);
    ee0[0] = __builtin_amdgcn_exp2f(ee0[0]);
    ee1[0] = acc[1][0][0] * -1.44269504f;
    SB();
    acc[3][1] = mfma16(W[2][1], B[0][1], acc[3][1]);
    SB();
    ee0[1] = __builtin_amdgcn_exp2f(ee0[1]);
    ee1[1] = acc[1][0][1] * -1.44269504f;
    SB();
    acc[2][0] = mfma16(W[3][0], B[0][2], acc[2][0]);
    SB();
    B[1][2] = *(const u32x4*)(hb0 + 38912);
    ee0[2] = __builtin_amdgcn_exp2f(ee0[2]);
    ee1[2] = acc[1][0][2] * -1.44269504f;
    SB();
    acc[2][1] = mfma16(W[3][1], B[0][2], acc[2][1]);
    SB();
    ee0[3] = __builtin_amdgcn_exp2f(ee0[3]);
    ee1[3] = acc[1][0][3] * -1.44269504f;
    SB();
    acc[3][0] = mfma16(W[3][0], B[0][3], acc[3][0]);
    SB();
    B[1][3] = *(const u32x4*)(hb0 + 39936);
    eg0[0] = acc[0][0][0] * acc[0][1][0];
    eg0[1] = acc[0][0][1] * acc[0][1][1];
    SB();
    acc[3][1] = mfma16(W[3][1], B[0][3], acc[3][1]);
    SB();
    eg0[2] = acc[0][0][2] * acc[0][1][2];
    eg0[3] = acc[0][0][3] * acc[0][1][3];
    SB();
    DMA_SC1(lds0 + L_E + wave * 4096 + 1024, l16, w2s + 26624);
    SB();
    acc[2][0] = mfma16(W[4][0], B[1][0], acc[2][0]);
    SB();
    B[0][0] = *(const u32x4*)(hb0 + 40960);
    ee0[0] += 1.f;
    ee0[1] += 1.f;
    SB();
    acc[2][1] = mfma16(W[4][1], B[1][0], acc[2][1]);
    SB();
    ee0[2] += 1.f;
    ee0[3] += 1.f;
    SB();
    acc[3][0] = mfma16(W[4][0], B[1][1], acc[3][0]);
    SB();
    B[0][1] = *(const u32x4*)(hb0 + 41984);
    ee1[0] = __builtin_amdgcn_exp2f(ee1[0]);
    eg1[0] = acc[1][0][0] * acc[1][1][0];
    SB();
    acc[3][1] = mfma16(W[4][1], B[1][1], acc[3][1]);
    SB();
    ee1[1] = __builtin_amdgcn_exp2f(ee1[1]);
    eg1[1] = acc[1][0][1] * acc[1][1][1];
    SB();
    acc[2][0] = mfma16(W[5][0], B[1][2], acc[2][0]);
    SB();
    B[0][2] = *(const u32x4*)(hb0 + 43008);
    ee1[2] = __builtin_amdgcn_exp2f(ee1[2]);
    eg1[2] = acc[1][0][2] * acc[1][1][2];
    SB();
    acc[2][1] = mfma16(W[5][1], B[1][2], acc[2][1]);
    SB();
    ee1[3] = __builtin_amdgcn_exp2f(ee1[3]);
    eg1[3] = acc[1][0][3] * acc[1][1][3];
    SB();
    acc[3][0] = mfma16(W[5][0], B[1][3], acc[3][0]);
    SB();
    B[0][3] = *(const u32x4*)(hb0 + 44032);
    ee0[0] = __builtin_amdgcn_rcpf(ee0[0]);
    ee0[1] = __builtin_amdgcn_rcpf(ee0[1]);
    SB();
    acc[3][1] = mfma16(W[5][1], B[1][3], acc[3][1]);
    SB();
    ee0[2] = __builtin_amdgcn_rcpf(ee0[2]);
    ee0[3] = __builtin_amdgcn_rcpf(ee0[3]);
    SB();
    DMA_SC1(lds0 + L_E + wave * 4096 + 2048, l16, w2s + 25600);
    SB();
    acc[2][0] = mfma16(W[6][0], B[0][0], acc[2][0]);
    SB();
    B[1][0] = *(const u32x4*)(hb0 + 45056);
    ee1[0] += 1.f;
    ee1[1] += 1.f;
    SB();
    acc[2][1] = mfma16(W[6][1], B[0][0], acc[2][1]);
    SB();
    ee1[2] += 1.f;
    ee1[3] += 1.f;
    SB();
    acc[3][0] = mfma16(W[6][0], B[0][1], acc[3][0]);
    SB();
    B[1][1] = *(const u32x4*)(hb0 + 46080);
    eg0[0] *= ee0[0];
    eg0[1] *= ee0[1];
    SB();
    acc[3][1] = mfma16(W[6][1], B[0][1], acc[3][1]);
    SB();
    eg0[2] *= ee0[2];
    eg0[3] *= ee0[3];
    SB();
    acc[2][0] = mfma16(W[7][0], B[0][2], acc[2][0]);
    SB();
    B[1][2] = *(const u32x4*)(hb0 + 47104);
    eo[0] = pk2(eg0[0], eg0[1]);
    eo[1] = pk2(eg0[2], eg0[3]);
    SB();
    acc[2][1] = mfma16(W[7][1], B[0][2], acc[2][1]);
    SB();
    ee1[0] = __builtin_amdgcn_rcpf(ee1[0]);
    ee1[1] = __builtin_amdgcn_rcpf(ee1[1]);
    SB();
    acc[3][0] = mfma16(W[7][0], B[0][3], acc[3][0]);
    SB();
    B[1][3] = *(const u32x4*)(hb0 + 48128);
    ee1[2] = __builtin_amdgcn_rcpf(ee1[2]);
    ee1[3] = __builtin_amdgcn_rcpf(ee1[3]);
    SB();
    acc[3][1] = mfma16(W[7][1], B[0][3], acc[3][1]);
    SB();
    eg1[0] *= ee1[0];
    eg1[1] *= ee1[1];
    SB();
    DMA_SC1(lds0 + L_E + wave * 4096 + 3072, l16, w2s + 24576);
    SB();
    acc[2][0] = mfma16(W[8][0], B[1][0], acc[2][0]);
    SB();
    B[0][0] = *(const u32x4*)(hb0 + 49152);
    eg1[2] *= ee1[2];
    eg1[3] *= ee1[3];
    SB();
    acc[2][1] = mfma16(W[8][1], B[1][0], acc[2][1]);
    SB();
    eo[2] = pk2(eg1[0], eg1[1]);
    eo[3] = pk2(eg1[2], eg1[3]);
    SB();
    acc[3][0] = mfma16(W[8][0], B[1][1], acc[3][0]);
    SB();
    B[0][1] = *(const u32x4*)(hb0 + 50176);
    EPI_STORE(0);
    SB();
    acc[3][1] = mfma16(W[8][1], B[1][1], acc[3][1]);
    SB();
    SB();
    acc[2][0] = mfma16(W[9][0], B[1][2], acc[2][0]);
    SB();
    B[0][2] = *(const u32x4*)(hb0 + 51200);
    SB();
    acc[2][1] = mfma16(W[9][1], B[1][2], acc[2][1]);
    SB();
    SB();
    acc[3][0] = mfma16(W[9][0], B[1][3], acc[3][0]);
    SB();
    B[0][3] = *(const u32x4*)(hb0 + 52224);
    SB();
    acc[3][1] = mfma16(W[9][1], B[1][3], acc[3][1]);
    SB();
    SB();
    BLD_SC1(W2[0][0], l16, w2s + 0, 0);
    SB();
    acc[2][0] = mfma16(W[10][0], B[0][0], acc[2][0]);
    SB();
    B[1][0] = *(const u32x4*)(hb0 + 53248);
    SB();
    acc[2][1] = mfma16(W[10][1], B[0][0], acc[2][1]);
    SB();
    SB();
    acc[3][0] = mfma16(W[10][0], B[0][1], acc[3][0]);
    SB();
    B[1][1] = *(const u32x4*)(hb0 + 54272);
    SB();
    acc[3][1] = mfma16(W[10][1], B[0][1], acc[3][1]);
    SB();
    SB();
    acc[2][0] = mfma16(W[11][0], B[0][2], acc[2][0]);
    SB();
    B[1][2] = *(const u32x4*)(hb0 + 55296);
    SB();
    acc[2][1] = mfma16(W[11][1], B[0][2], acc[2][1]);
    SB();
    SB();
    acc[3][0] = mfma16(W[11][0], B[0][3], acc[3][0]);
    SB();
    B[1][3] = *(const u32x4*)(hb0 + 56320);
    SB();
    acc[3][1] = mfma16(W[11][1], B[0][3], acc[3][1]);
    SB();
    SB();
    BLD_SC1(W2[0][1], l16, w2s + 0, 1024);
    SB();
    acc[2][0] = mfma16(W[12][0], B[1][0], acc[2][0]);
    SB();
    B[0][0] = *(const u32x4*)(hb0 + 57344);
    SB();
    acc[2][1] = mfma16(W[12][1], B[1][0], acc[2][1]);
    SB();
    SB();
    acc[3][0] = mfma16(W[12][0], B[1][1], acc[3][0]);
    SB();
    B[0][1] = *(const u32x4*)(hb0 + 58368);
    SB();
    acc[3][1] = mfma16(W[12][1], B[1][1], acc[3][1]);
    SB();
    SB();
    acc[2][0] = mfma16(W[13][0], B[1][2], acc[2][0]);
    SB();
    B[0][2] = *(const u32x4*)(hb0 + 59392);
    SB();
    acc[2][1] = mfma16(W[13][1], B[1][2], acc[2][1]);
    SB();
    SB();
    acc[3][0] = mfma16(W[13][0], B[1][3], acc[3][0]);
    SB();
    B[0][3] = *(const u32x4*)(hb0 + 60416);
    SB();
    acc[3][1] = mfma16(W[13][1], B[1][3], acc[3][1]);
    SB();
    SB();
    BLD_SC1(W2[0][2], l16, w2s + 0, 2048);
    SB();
    acc[4][0] = mfma16(W[0][0], B[0][0], acc[4][0]);
    SB();
    B[1][0] = *(const u32x4*)(hb0 + 61440);
    SB();
    acc[4][1] = mfma16(W[0][1], B[0][0], acc[4][1]);
    SB();
    SB();
    acc[5][0] = mfma16(W[0][0], B[0][1], acc[5][0]);
    SB();
    B[1][1] = *(const u32x4*)(hb0 + 62464);
    SB();
    acc[5][1] = mfma16(W[0][1], B[0][1], acc[5][1]);
    SB();
    SB();
    acc[4][0] = mfma16(W[1][0], B[0][2], acc[4][0]);
    SB();
    B[1][2] = *(const u32x4*)(hb0 + 63488);
    SB();
    acc[4][1] = mfma16(W[1][1], B[0][2], acc[4][1]);
    SB();
    SB();
    acc[5][0] = mfma16(W[1][0], B[0][3], acc[5][0]);
    SB();
    B[1][3] = *(const u32x4*)(hb0 + 64512);
    SB();
    acc[5][1] = mfma16(W[1][1], B[0][3], acc[5][1]);
    SB();
    SB();
    BLD_SC1(W2[0][3], l16, w2s + 0, 3072);
    SB();
    acc[4][0] = mfma16(W[2][0], B[1][0], acc[4][0]);
    SB();
    B[0][0] = *(const LAS u32x4*)(hb1 + 4096);
    ee2[0] = acc[2][0][0] * -1.44269504f;
    ee2[1] = acc[2][0][1] * -1.44269504f;
    SB();
    acc[4][1] = mfma16(W[2][1], B[1][0], acc[4][1]);
    SB();
    ee2[2] = acc[2][0][2] * -1.44269504f;
    ee2[3] = acc[2][0][3] * -1.44269504f;
    SB();
    acc[5][0] = mfma16(W[2][0], B[1][1], acc[5][0]);
    SB();
    B[0][1] = *(const LAS u32x4*)(hb1 + 5120);
    ee2[0] = __builtin_amdgcn_exp2f(ee2[0]);
    ee0[0] = acc[3][0][0] * -1.44269504f;
    SB();
    acc[5][1] = mfma16(W[2][1], B[1][1], acc[5][1]);
    SB();
    ee2[1] = __builtin_amdgcn_exp2f(ee2[1]);
    ee0[1] = acc[3][0][1] * -1.44269504f;
    SB();
    acc[4][0] = mfma16(W[3][0], B[1][2], acc[4][0]);
    SB();
    B[0][2] = *(const LAS u32x4*)(hb1 + 6144);
    ee2[2] = __builtin_amdgcn_exp2f(ee2[2]);
    ee0[2] = acc[3][0][2] * -1.44269504f;
    SB();
    acc[4][1] = mfma16(W[3][1], B[1][2], acc[4][1]);
    SB();
    ee2[3] = __builtin_amdgcn_exp2f(ee2[3]);
    ee0[3] = acc[3][0][3] * -1.44269504f;
    SB();
    acc[5][0] = mfma16(W[3][0], B[1][3], acc[5][0]);
    SB();
    B[0][3] = *(const LAS u32x4*)(hb1 + 7168);
    eg2[0] = acc[2][0][0] * acc[2][1][0];
    eg2[1] = acc[2][0][1] * acc[2][1][1];
    SB();
    acc[5][1] = mfma16(W[3][1], B[1][3], acc[5][1]);
    SB();
    eg2[2] = acc[2][0][2] * acc[2][1][2];
    eg2[3] = acc[2][0][3] * acc[2][1][3];
    SB();
    BLD_SC1(W2[1][0], l16, w2s + 4096, 0);
    SB();
    acc[4][0] = mfma16(W[4][0], B[0][0], acc[4][0]);
    SB();
    B[1][0] = *(const LAS u32x4*)(hb1 + 8192);
    ee2[0] += 1.f;
    ee2[1] += 1.f;
    SB();
    acc[4][1] = mfma16(W[4][1], B[0][0], acc[4][1]);
    SB();
    ee2[2] += 1.f;
    ee2[3] += 1.f;
    SB();
    acc[5][0] = mfma16(W[4][0], B[0][1], acc[5][0]);
    SB();
    B[1][1] = *(const LAS u32x4*)(hb1 + 9216);
    ee0[0] = __builtin_amdgcn_exp2f(ee0[0]);
    eg0[0] = acc[3][0][0] * acc[3][1][0];
    SB();
    acc[5][1] = mfma16(W[4][1], B[0][1], acc[5][1]);
    SB();
    ee0[1] = __builtin_amdgcn_exp2f(ee0[1]);
    eg0[1] = acc[3][0][1] * acc[3][1][1];
    SB();
    acc[4][0] = mfma16(W[5][0], B[0][2], acc[4][0]);
    SB();
    B[1][2] = *(const LAS u32x4*)(hb1 + 10240);
    ee0[2] = __builtin_amdgcn_exp2f(ee0[2]);
    eg0[2] = acc[3][0][2] * acc[3][1][2];
    SB();
    acc[4][1] = mfma16(W[5][1], B[0][2], acc[4][1]);
    SB();
    ee0[3] = __builtin_amdgcn_exp2f(ee0[3]);
    eg0[3] = acc[3][0][3] * acc[3][1][3];
    SB();
    acc[5][0] = mfma16(W[5][0], B[0][3], acc[5][0]);
    SB();
    B[1][3] = *(const LAS u32x4*)(hb1 + 11264);
    ee2[0] = __builtin_amdgcn_rcpf(ee2[0]);
    ee2[1] = __builtin_amdgcn_rcpf(ee2[1]);
    SB();
    acc[5][1] = mfma16(W[5][1], B[0][3], acc[5][1]);
    SB();
    ee2[2] = __builtin_amdgcn_rcpf(ee2[2]);
    ee2[3] = __builtin_amdgcn_rcpf(ee2[3]);
    SB();
    WAITV(11);
    BAR();
    BLD_SC1(W2[1][1], l16, w2s + 4096, 1024);
    SB();
    acc[4][0] = mfma16(W[6][0], B[1][0], acc[4][0]);
    SB();
    B[0][0] = *(const LAS u32x4*)(hb1 + 12288);
    ee0[0] += 1.f;
    ee0[1] += 1.f;
    SB();
    acc[4][1] = mfma16(W[6][1], B[1][0], acc[4][1]);
    SB();
    ee0[2] += 1.f;
    ee0[3] += 1.f;
    SB();
    acc[5][0] = mfma16(W[6][0], B[1][1], acc[5][0]);
    SB();
    B[0][1] = *(const LAS u32x4*)(hb1 + 13312);
    eg2[0] *= ee2[0];
    eg2[1] *= ee2[1];
    SB();
    acc[5][1] = mfma16(W[6][1], B[1][1], acc[5][1]);
    SB();
    eg2[2] *= ee2[2];
    eg2[3] *= ee2[3];
    SB();
    acc[4][0] = mfma16(W[7][0], B[1][2], acc[4][0]);
    SB();
    B[0][2] = *(const LAS u32x4*)(hb1 + 14336);
    eo[0] = pk2(eg2[0], eg2[1]);
    eo[1] = pk2(eg2[2], eg2[3]);
    SB();
    acc[4][1] = mfma16(W[7][1], B[1][2], acc[4][1]);
    SB();
    ee0[0] = __builtin_amdgcn_rcpf(ee0[0]);
    ee0[1] = __builtin_amdgcn_rcpf(ee0[1]);
    SB();
    acc[5][0] = mfma16(W[7][0], B[1][3], acc[5][0]);
    SB();
    B[0][3] = *(const LAS u32x4*)(hb1 + 15360);
    ee0[2] = __builtin_amdgcn_rcpf(ee0[2]);
    ee0[3] = __builtin_amdgcn_rcpf(ee0[3]);
    SB();
    acc[5][1] = mfma16(W[7][1], B[1][3], acc[5][1]);
    SB();
    eg0[0] *= ee0[0];
    eg0[1] *= ee0[1];
    SB();
    BLD_SC1(W2[1][2], l16, w2s + 4096, 2048);
    SB();
    acc[4][0] = mfma16(W[8][0], B[0][0], acc[4][0]);
    SB();
    B[1][0] = *(const LAS u32x4*)(hb1 + 16384);
    eg0[2] *= ee0[2];
    eg0[3] *= ee0[3];
    SB();
    acc[4][1] = mfma16(W[8][1], B[0][0], acc[4][1]);
    SB();
    eo[2] = pk2(eg0[0], eg0[1]);
    eo[3] = pk2(eg0[2], eg0[3]);
    SB();
    acc[5][0] = mfma16(W[8][0], B[0][1], acc[5][0]);
    SB();
    B[1][1] = *(const LAS u32x4*)(hb1 + 17408);
    EPI_STORE(1);
    SB();
    acc[5][1] = mfma16(W[8][1], B[0][1], acc[5][1]);
    SB();
    SB();
    acc[4][0] = mfma16(W[9][0], B[0][2], acc[4][0]);
    SB();
    B[1][2] = *(const LAS u32x4*)(hb1 + 18432);
    SB();
    acc[4][1] = mfma16(W[9][1], B[0][2], acc[4][1]);
    SB();
    SB();
    acc[5][0] = mfma16(W[9][0], B[0][3], acc[5][0]);
    SB();
    B[1][3] = *(const LAS u32x4*)(hb1 + 19456);
    SB();
    acc[5][1] = mfma16(W[9][1], B[0][3], acc[5][1]);
    SB();
    SB();
    BLD_SC1(W2[1][3], l16, w2s + 4096, 3072);
    SB();
    acc[4][0] = mfma16(W[10][0], B[1][0], acc[4][0]);
    SB();
    B[0][0] = *(const LAS u32x4*)(hb1 + 20480);
    SB();
    acc[4][1] = mfma16(W[10][1], B[1][0], acc[4][1]);
    SB();
    SB();
    acc[5][0] = mfma16(W[10][0], B[1][1], acc[5][0]);
    SB();
    B[0][1] = *(const LAS u32x4*)(hb1 + 21504);
    SB();
    acc[5][1] = mfma16(W[10][1], B[1][1], acc[5][1]);
    SB();
    SB();
    acc[4][0] = mfma16(W[11][0], B[1][2], acc[4][0]);
    SB();
    B[0][2] = *(const LAS u32x4*)(hb1 + 22528);
    SB();
    acc[4][1] = mfma16(W[11][1], B[1][2], acc[4][1]);
    SB();
    SB();
    acc[5][0] = mfma16(W[11][0], B[1][3], acc[5][0]);
    SB();
    B[0][3] = *(const LAS u32x4*)(hb1 + 23552);
    SB();
    acc[5][1] = mfma16(W[11][1], B[1][3], acc[5][1]);
    SB();
    SB();
    SB();
    acc[4][0] = mfma16(W[12][0], B[0][0], acc[4][0]);
    SB();
    B[1][0] = *(const LAS u32x4*)(hb1 + 24576);
    SB();
    acc[4][1] = mfma16(W[12][1], B[0][0], acc[4][1]);
    SB();
    SB();
    acc[5][0] = mfma16(W[12][0], B[0][1], acc[5][0]);
    SB();
    B[1][1] = *(const LAS u32x4*)(hb1 + 25600);
    SB();
    acc[5][1] = mfma16(W[12][1], B[0][1], acc[5][1]);
    SB();
    SB();
    acc[4][0] = mfma16(W[13][0], B[0][2], acc[4][0]);
    SB();
    B[1][2] = *(const LAS u32x4*)(hb1 + 26624);
    SB();
    acc[4][1] = mfma16(W[13][1], B[0][2], acc[4][1]);
    SB();
    SB();
    acc[5][0] = mfma16(W[13][0], B[0][3], acc[5][0]);
    SB();
    B[1][3] = *(const LAS u32x4*)(hb1 + 27648);
    SB();
    acc[5][1] = mfma16(W[13][1], B[0][3], acc[5][1]);
    SB();
    SB();
    SB();
    acc[6][0] = mfma16(W[0][0], B[1][0], acc[6][0]);
    SB();
    B[0][0] = *(const LAS u32x4*)(hb1 + 28672);
    SB();
    acc[6][1] = mfma16(W[0][1], B[1][0], acc[6][1]);
    SB();
    SB();
    acc[7][0] = mfma16(W[0][0], B[1][1], acc[7][0]);
    SB();
    B[0][1] = *(const LAS u32x4*)(hb1 + 29696);
    SB();
    acc[7][1] = mfma16(W[0][1], B[1][1], acc[7][1]);
    SB();
    SB();
    acc[6][0] = mfma16(W[1][0], B[1][2], acc[6][0]);
    SB();
    B[0][2] = *(const LAS u32x4*)(hb1 + 30720);
    SB();
    acc[6][1] = mfma16(W[1][1], B[1][2], acc[6][1]);
    SB();
    SB();
    acc[7][0] = mfma16(W[1][0], B[1][3], acc[7][0]);
    SB();
    B[0][3] = *(const LAS u32x4*)(hb1 + 31744);
    SB();
    acc[7][1] = mfma16(W[1][1], B[1][3], acc[7][1]);
    SB();
    SB();
    WAITV(1);
    ARRIVE(0);
    BLD_SC1(W2[2][0], l16, w2s + 8192, 0);
    BLD_SC1(W2[2][1], l16, w2s + 8192, 1024);
    BLD_SC1(W2[2][2], l16, w2s + 8192, 2048);
    SB();
    acc[6][0] = mfma16(W[2][0], B[0][0], acc[6][0]);
    SB();
    B[1][0] = *(const LAS u32x4*)(hb1 + 32768);
    ee1[0] = acc[4][0][0] * -1.44269504f;
    ee1[1] = acc[4][0][1] * -1.44269504f;
    SB();
    acc[6][1] = mfma16(W[2][1], B[0][0], acc[6][1]);
    SB();
    ee1[2] = acc[4][0][2] * -1.44269504f;
    ee1[3] = acc[4][0][3] * -1.44269504f;
    SB();
    acc[7][0] = mfma16(W[2][0], B[0][1], acc[7][0]);
    SB();
    B[1][1] = *(const LAS u32x4*)(hb1 + 33792);
    ee1[0] = __builtin_amdgcn_exp2f(ee1[0]);
    ee2[0] = acc[5][0][0] * -1.44269504f;
    SB();
    acc[7][1] = mfma16(W[2][1], B[0][1], acc[7][1]);
    SB();
    ee1[1] = __builtin_amdgcn_exp2f(ee1[1]);
    ee2[1] = acc[5][0][1] * -1.44269504f;
    SB();
    acc[6][0] = mfma16(W[3][0], B[0][2], acc[6][0]);
    SB();
    B[1][2] = *(const LAS u32x4*)(hb1 + 34816);
    ee1[2] = __builtin_amdgcn_exp2f(ee1[2]);
    ee2[2] = acc[5][0][2] * -1.44269504f;
    SB();
    acc[6][1] = mfma16(W[3][1], B[0][2], acc[6][1]);
    SB();
    ee1[3] = __builtin_amdgcn_exp2f(ee1[3]);
    ee2[3] = acc[5][0][3] * -1.44269504f;
    SB();
    acc[7][0] = mfma16(W[3][0], B[0][3], acc[7][0]);
    SB();
    B[1][3] = *(const LAS u32x4*)(hb1 + 35840);
    eg1[0] = acc[4][0][0] * acc[4][1][0];
    eg1[1] = acc[4][0][1] * acc[4][1][1];
    SB();
    acc[7][1] = mfma16(W[3][1], B[0][3], acc[7][1]);
    SB();
    eg1[2] = acc[4][0][2] * acc[4][1][2];
    eg1[3] = acc[4][0][3] * acc[4][1][3];
    SB();
    BLD_SC1(W2[2][3], l16, w2s + 8192, 3072);
    BLD_SC1(W2[3][0], l16, w2s + 12288, 0);
    BLD_SC1(W2[3][1], l16, w2s + 12288, 1024);
    SB();
    acc[6][0] = mfma16(W[4][0], B[1][0], acc[6][0]);
    SB();
    B[0][0] = *(const LAS u32x4*)(hb1 + 36864);
    ee1[0] += 1.f;
    ee1[1] += 1.f;
    SB();
    acc[6][1] = mfma16(W[4][1], B[1][0], acc[6][1]);
    SB();
    ee1[2] += 1.f;
    ee1[3] += 1.f;
    SB();
    acc[7][0] = mfma16(W[4][0], B[1][1], acc[7][0]);
    SB();
    B[0][1] = *(const LAS u32x4*)(hb1 + 37888);
    ee2[0] = __builtin_amdgcn_exp2f(ee2[0]);
    eg2[0] = acc[5][0][0] * acc[5][1][0];
    SB();
    acc[7][1] = mfma16(W[4][1], B[1][1], acc[7][1]);
    SB();
    ee2[1] = __builtin_amdgcn_exp2f(ee2[1]);
    eg2[1] = acc[5][0][1] * acc[5][1][1];
    SB();
    acc[6][0] = mfma16(W[5][0], B[1][2], acc[6][0]);
    SB();
    B[0][2] = *(const LAS u32x4*)(hb1 + 38912);
    ee2[2] = __builtin_amdgcn_exp2f(ee2[2]);
    eg2[2] = acc[5][0][2] * acc[5][1][2];
    SB();
    acc[6][1] = mfma16(W[5][1], B[1][2], acc[6][1]);
    SB();
    ee2[3] = __builtin_amdgcn_exp2f(ee2[3]);
    eg2[3] = acc[5][0][3] * acc[5][1][3];
    SB();
    acc[7][0] = mfma16(W[5][0], B[1][3], acc[7][0]);
    SB();
    B[0][3] = *(const LAS u32x4*)(hb1 + 39936);
    ee1[0] = __builtin_amdgcn_rcpf(ee1[0]);
    ee1[1] = __builtin_amdgcn_rcpf(ee1[1]);
    SB();
    acc[7][1] = mfma16(W[5][1], B[1][3], acc[7][1]);
    SB();
    ee1[2] = __builtin_amdgcn_rcpf(ee1[2]);
    ee1[3] = __builtin_amdgcn_rcpf(ee1[3]);
    SB();
    BLD_SC1(W2[3][2], l16, w2s + 12288, 2048);
    BLD_SC1(W2[3][3], l16, w2s + 12288, 3072);
    BLD_SC1(W2[4][0], l16, w2s + 16384, 0);
    SB();
    acc[6][0] = mfma16(W[6][0], B[0][0], acc[6][0]);
    SB();
    B[1][0] = *(const LAS u32x4*)(hb1 + 40960);
    ee2[0] += 1.f;
    ee2[1] += 1.f;
    SB();
    acc[6][1] = mfma16(W[6][1], B[0][0], acc[6][1]);
    SB();
    ee2[2] += 1.f;
    ee2[3] += 1.f;
    SB();
    acc[7][0] = mfma16(W[6][0], B[0][1], acc[7][0]);
    SB();
    B[1][1] = *(const LAS u32x4*)(hb1 + 41984);
    eg1[0] *= ee1[0];
    eg1[1] *= ee1[1];
    SB();
    acc[7][1] = mfma16(W[6][1], B[0][1], acc[7][1]);
    SB();
    eg1[2] *= ee1[2];
    eg1[3] *= ee1[3];
    SB();
    acc[6][0] = mfma16(W[7][0], B[0][2], acc[6][0]);
    SB();
    B[1][2] = *(const LAS u32x4*)(hb1 + 43008);
    eo[0] = pk2(eg1[0], eg1[1]);
    eo[1] = pk2(eg1[2], eg1[3]);
    SB();
    acc[6][1] = mfma16(W[7][1], B[0][2], acc[6][1]);
    SB();
    ee2[0] = __builtin_amdgcn_rcpf(ee2[0]);
    ee2[1] = __builtin_amdgcn_rcpf(ee2[1]);
    SB();
    acc[7][0] = mfma16(W[7][0], B[0][3], acc[7][0]);
    SB();
    B[1][3] = *(const LAS u32x4*)(hb1 + 44032);
    ee2[2] = __builtin_amdgcn_rcpf(ee2[2]);
    ee2[3] = __builtin_amdgcn_rcpf(ee2[3]);
    SB();
    acc[7][1] = mfma16(W[7][1], B[0][3], acc[7][1]);
    SB();
    eg2[0] *= ee2[0];
    eg2[1] *= ee2[1];
    SB();
    BLD_SC1(W2[4][1], l16, w2s + 16384, 1024);
    BLD_SC1(W2[4][2], l16, w2s + 16384, 2048);
    BLD_SC1(W2[4][3], l16, w2s + 16384, 3072);
    SB();
    acc[6][0] = mfma16(W[8][0], B[1][0], acc[6][0]);
    SB();
    B[0][0] = *(const LAS u32x4*)(hb1 + 45056);
    eg2[2] *= ee2[2];
    eg2[3] *= ee2[3];
    SB();
    acc[6][1] = mfma16(W[8][1], B[1][0], acc[6][1]);
    SB();
    eo[2] = pk2(eg2[0], eg2[1]);
    eo[3] = pk2(eg2[2], eg2[3]);
    SB();
    acc[7][0] = mfma16(W[8][0], B[1][1], acc[7][0]);
    SB();
    B[0][1] = *(const LAS u32x4*)(hb1 + 46080);
    EPI_STORE(2);
    SB();
    acc[7][1] = mfma16(W[8][1], B[1][1], acc[7][1]);
    SB();
    SB();
    acc[6][0] = mfma16(W[9][0], B[1][2], acc[6][0]);
    SB();
    B[0][2] = *(const LAS u32x4*)(hb1 + 47104);
    SB();
    acc[6][1] = mfma16(W[9][1], B[1][2], acc[6][1]);
    SB();
    SB();
    acc[7][0] = mfma16(W[9][0], B[1][3], acc[7][0]);
    SB();
    B[0][3] = *(const LAS u32x4*)(hb1 + 48128);
    SB();
    acc[7][1] = mfma16(W[9][1], B[1][3], acc[7][1]);
    SB();
    SB();
    BLD_SC1(W2[5][0], l16, w2s + 20480, 0);
    BLD_SC1(W2[5][1], l16, w2s + 20480, 1024);
    BLD_SC1(W2[5][2], l16, w2s + 20480, 2048);
    SB();
    acc[6][0] = mfma16(W[10][0], B[0][0], acc[6][0]);
    SB();
    B[1][0] = *(const LAS u32x4*)(hb1 + 49152);
    SB();
    acc[6][1] = mfma16(W[10][1], B[0][0], acc[6][1]);
    SB();
    SB();
    acc[7][0] = mfma16(W[10][0], B[0][1], acc[7][0]);
    SB();
    B[1][1] = *(const LAS u32x4*)(hb1 + 50176);
    SB();
    acc[7][1] = mfma16(W[10][1], B[0][1], acc[7][1]);
    SB();
    SB();
    acc[6][0] = mfma16(W[11][0], B[0][2], acc[6][0]);
    SB();
    B[1][2] = *(const LAS u32x4*)(hb1 + 51200);
    SB();
    acc[6][1] = mfma16(W[11][1], B[0][2], acc[6][1]);
    SB();
    SB();
    acc[7][0] = mfma16(W[11][0], B[0][3], acc[7][0]);
    SB();
    B[1][3] = *(const LAS u32x4*)(hb1 + 52224);
    SB();
    acc[7][1] = mfma16(W[11][1], B[0][3], acc[7][1]);
    SB();
    SB();
    BLD_SC1(W2[5][3], l16, w2s + 20480, 3072);
    SB();
    acc[6][0] = mfma16(W[12][0], B[1][0], acc[6][0]);
    SB();
    SB();
    acc[6][1] = mfma16(W[12][1], B[1][0], acc[6][1]);
    SB();
    SB();
    acc[7][0] = mfma16(W[12][0], B[1][1], acc[7][0]);
    SB();
    SB();
    acc[7][1] = mfma16(W[12][1], B[1][1], acc[7][1]);
    SB();
    SB();
    acc[6][0] = mfma16(W[13][0], B[1][2], acc[6][0]);
    SB();
    SB();
    acc[6][1] = mfma16(W[13][1], B[1][2], acc[6][1]);
    SB();
    SB();
    acc[7][0] = mfma16(W[13][0], B[1][3], acc[7][0]);
    SB();
    SB();
    acc[7][1] = mfma16(W[13][1], B[1][3], acc[7][1]);
    SB();
    SB();
    ee0[0] = acc[6][0][0] * -1.44269504f;
    ee0[1] = acc[6][0][1] * -1.44269504f;
    ee0[2] = acc[6][0][2] * -1.44269504f;
    ee0[3] = acc[6][0][3] * -1.44269504f;
    SB();
    ee0[0] = __builtin_amdgcn_exp2f(ee0[0]);
    ee1[0] = acc[7][0][0] * -1.44269504f;
    ee0[1] = __builtin_amdgcn_exp2f(ee0[1]);
    ee1[1] = acc[7][0][1] * -1.44269504f;
    SB();
    ee0[2] = __builtin_amdgcn_exp2f(ee0[2]);
    ee1[2] = acc[7][0][2] * -1.44269504f;
    ee0[3] = __builtin_amdgcn_exp2f(ee0[3]);
    ee1[3] = acc[7][0][3] * -1.44269504f;
    SB();
    eg0[0] = acc[6][0][0] * acc[6][1][0];
    eg0[1] = acc[6][0][1] * acc[6][1][1];
    eg0[2] = acc[6][0][2] * acc[6][1][2];
    eg0[3] = acc[6][0][3] * acc[6][1][3];
    SB();
    ee0[0] += 1.f;
    ee0[1] += 1.f;
    ee0[2] += 1.f;
    ee0[3] += 1.f;
    SB();
    ee1[0] = __builtin_amdgcn_exp2f(ee1[0]);
    eg1[0] = acc[7][0][0] * acc[7][1][0];
    ee1[1] = __builtin_amdgcn_exp2f(ee1[1]);
    eg1[1] = acc[7][0][1] * acc[7][1][1];
    SB();
    ee1[2] = __builtin_amdgcn_exp2f(ee1[2]);
    eg1[2] = acc[7][0][2] * acc[7][1][2];
    ee1[3] = __builtin_amdgcn_exp2f(ee1[3]);
    eg1[3] = acc[7][0][3] * acc[7][1][3];
    SB();
    ee0[0] = __builtin_amdgcn_rcpf(ee0[0]);
    ee0[1] = __builtin_amdgcn_rcpf(ee0[1]);
    ee0[2] = __builtin_amdgcn_rcpf(ee0[2]);
    ee0[3] = __builtin_amdgcn_rcpf(ee0[3]);
    SB();
    ee1[0] += 1.f;
    ee1[1] += 1.f;
    ee1[2] += 1.f;
    ee1[3] += 1.f;
    SB();
    eg0[0] *= ee0[0];
    eg0[1] *= ee0[1];
    eg0[2] *= ee0[2];
    eg0[3] *= ee0[3];
    SB();
    eo[0] = pk2(eg0[0], eg0[1]);
    eo[1] = pk2(eg0[2], eg0[3]);
    ee1[0] = __builtin_amdgcn_rcpf(ee1[0]);
    ee1[1] = __builtin_amdgcn_rcpf(ee1[1]);
    SB();
    ee1[2] = __builtin_amdgcn_rcpf(ee1[2]);
    ee1[3] = __builtin_amdgcn_rcpf(ee1[3]);
    eg1[0] *= ee1[0];
    eg1[1] *= ee1[1];
    SB();
    eg1[2] *= ee1[2];
    eg1[3] *= ee1[3];
    eo[2] = pk2(eg1[0], eg1[1]);
    eo[3] = pk2(eg1[2], eg1[3]);
    SB();
    EPI_STORE(3);
    SB();
    STORED();
    WAITV(28);
    W2[6][3] = *(const u32x4*)(smem + L_E + wave * 4096 + 0 + l16);
    W2[6][2] = *(const u32x4*)(smem + L_E + wave * 4096 + 1024 + l16);
    W2[6][1] = *(const u32x4*)(smem + L_E + wave * 4096 + 2048 + l16);
    W2[6][0] = *(const u32x4*)(smem + L_E + wave * 4096 + 3072 + l16);
    WAIT_F(0);
    BLD_SC1(Af[0], l16, as0 + 0, 0);
    BLD_SC1(Af[1], l16, as0 + 0, 1024);
    BLD_SC1(Af[2], l16, as0 + 0, 2048);
    BLD_SC1(Af[3], l16, as0 + 0, 3072);
    WAITV(4);
    ARRIVE(2);
    BLD_SC1(Af[4], l16, as0 + 4096, 0);
    BLD_SC1(Af[5], l16, as0 + 4096, 1024);
    CC_ZERO();
    waitv1<5>(Af[0]);
    asm volatile("" : "+v"(W2[0][0]), "+v"(W2[0][1]), "+v"(W2[0][2]), "+v"(W2[0][3]));
    G2MM(0); SB();
    BLD_SC1(Af[6], l16, as0 + 4096, 2048);
    waitv1<5>(Af[1]);
    asm volatile("" : "+v"(W2[1][0]), "+v"(W2[1][1]), "+v"(W2[1][2]), "+v"(W2[1][3]));
    G2MM(1); SB();
    BLD_SC1(Af[7], l16, as0 + 114688, 0);
    waitv1<5>(Af[2]);
    asm volatile("" : "+v"(W2[2][0]), "+v"(W2[2][1]), "+v"(W2[2][2]), "+v"(W2[2][3]));
    G2MM(2); SB();
    BLD_SC1(Af[8], l16, as0 + 114688, 1024);
    BLD_SC1(x1w, (u32)(((srow >> 3) * 7 + db) * 1024 + (dl + (srow & 7)) * 16), OFF_X1, 0);
    waitv1<6>(Af[3]);
    asm volatile("" : "+v"(W2[3][0]), "+v"(W2[3][1]), "+v"(W2[3][2]), "+v"(W2[3][3]));
    G2MM(3); SB();
    BLD_SC1(Af[9], l16, as0 + 114688, 2048);
    waitv1<6>(Af[4]);
    asm volatile("" : "+v"(W2[4][0]), "+v"(W2[4][1]), "+v"(W2[4][2]), "+v"(W2[4][3]));
    G2MM(4); SB();
    BLD_SC1(Af[10], l16, as0 + 114688, 3072);
    waitv1<6>(Af[5]);
    asm volatile("" : "+v"(W2[5][0]), "+v"(W2[5][1]), "+v"(W2[5][2]), "+v"(W2[5][3]));
    G2MM(5); SB();
    BLD_SC1(Af[11], l16, as0 + 118784, 0);
    waitv1<6>(Af[6]);
    asm volatile("" : "+v"(W2[6][0]), "+v"(W2[6][1]), "+v"(W2[6][2]), "+v"(W2[6][3]));
    G2MM(6); SB();
    lds_wait(lcnt + 9, 8u);
    RED_WRITE(0); SB();
    BLD_SC1(Af[12], l16, as0 + 118784, 1024);
    CC_ZERO();
    waitv1<6>(Af[7]);
    G2MM(7); SB();
    BLD_SC1(Af[13], l16, as0 + 118784, 2048);
    waitv1<6>(Af[8]);
    G2MM(8); SB();
    WAIT_F(2);
    BLD_SC1(Af[14], l16, as0 + 229376, 0);
    waitv1<5>(Af[9]);
    G2MM(9); SB();
    BLD_SC1(Af[15], l16, as0 + 229376, 1024);
    waitv1<5>(Af[10]);
    G2MM(10); SB();
    BLD_SC1(Af[16], l16, as0 + 229376, 2048);
    waitv1<5>(Af[11]);
    G2MM(11); SB();
    BLD_SC1(Af[17], l16, as0 + 229376, 3072);
    waitv1<5>(Af[12]);
    G2MM(12); SB();
    BLD_SC1(Af[18], l16, as0 + 233472, 0);
    waitv1<5>(Af[13]);
    G2MM(13); SB();
    RED_WRITE(1); SB();
    BLD_SC1(Af[19], l16, as0 + 233472, 1024);
    CC_ZERO();
    waitv1<5>(Af[14]);
    G2MM(14); SB();
    BLD_SC1(Af[20], l16, as0 + 233472, 2048);
    waitv1<5>(Af[15]);
    G2MM(15); SB();
    BLD_SC1(Af[21], l16, as0 + 344064, 0);
    waitv1<5>(Af[16]);
    G2MM(16); SB();
    BLD_SC1(Af[22], l16, as0 + 344064, 1024);
    waitv1<5>(Af[17]);
    G2MM(17); SB();
    BLD_SC1(Af[23], l16, as0 + 344064, 2048);
    waitv1<5>(Af[18]);
    G2MM(18); SB();
    BLD_SC1(Af[24], l16, as0 + 344064, 3072);
    waitv1<5>(Af[19]);
    G2MM(19); SB();
    BLD_SC1(Af[25], l16, as0 + 348160, 0);
    waitv1<5>(Af[20]);
    G2MM(20); SB();
    RED_WRITE(2); SB();
    BLD_SC1(Af[26], l16, as0 + 348160, 1024);
    CC_ZERO();
    waitv1<5>(Af[21]);
    asm volatile("" : "+v"(x1w), "+v"(gbw));
    G2MM(21); SB();
    FINPREP();
    BLD_SC1(Af[27], l16, as0 + 348160, 2048);
    waitv1<5>(Af[22]);
    G2MM(22); SB();
    waitv1<4>(Af[23]);
    G2MM(23); SB();
    waitv1<3>(Af[24]);
    G2MM(24); SB();
    waitv1<2>(Af[25]);
    G2MM(25); SB();
    waitv1<1>(Af[26]);
    G2MM(26); SB();
    waitv1<0>(Af[27]);
    G2MM(27); SB();
    RED_WRITE(3); SB();
    {
        BAR();
        f32x4 p0[8], p1[8];
#pragma unroll
        for (int w = 0; w < 8; ++w) { p0[w] = *(const LAS f32x4*)(size_t)ra[w]; p1[w] = *(const LAS f32x4*)(size_t)(ra[w] + 256); }
        SB();
        const f32x4 v0 = ((p0[0] + p0[1]) + (p0[2] + p0[3])) + ((p0[4] + p0[5]) + (p0[6] + p0[7])), v1 = ((p1[0] + p1[1]) + (p1[2] + p1[3])) + ((p1[4] + p1[5]) + (p1[6] + p1[7]));
        float o[8];
#pragma unroll
        for (int e = 0; e < 8; ++e) o[e] = xf[e] + rbf(rbf(e < 4 ? v0[e & 3] : v1[e & 3]) * gf[e]);
        const u32x4 ov = {pk2(o[0], o[1]), pk2(o[2], o[3]), pk2(o[4], o[5]), pk2(o[6], o[7])};
        asm volatile("buffer_store_dwordx4 %0, %1, %2, 0 offen\n\ts_nop 1" :: "v"(ov), "v"(oo), "s"(srdo) : "memory");
    }
}

extern "C" void kernel_launch(void* const* d_in, const int* in_sizes, int n_in,
                              void* d_out, int out_size, void* d_ws, size_t ws_size,
                              hipStream_t stream) {
    (void)hipFuncSetAttribute((const void*)fused, hipFuncAttributeMaxDynamicSharedMemorySize, LDS_BYTES);
    fused<<<256, 512, LDS_BYTES, stream>>>((const u16*)d_in[0], (const u16*)d_in[1], (const u16*)d_in[2], (const u16*)d_in[3], (const u16*)d_in[4], (const u16*)d_in[5],
                                           (const u16*)d_in[7], (const u16*)d_in[8], (const u16*)d_in[9], (const u16*)d_in[10], (const u16*)d_in[11], (char*)d_ws, (u16*)d_out);
}
```
